```python
import torch

def _softmax(x, axis=-1):
    return torch.softmax(x, dim=axis)

def _gelu(x):
    return torch.nn.functional.gelu(x, approximate='tanh')

def _silu(x):
    return torch.nn.functional.silu(x)

def _sigmoid(x):
    return torch.sigmoid(x)

def _layernorm(x, axis=-1, eps=1e-05):
    m = x.mean(dim=axis, keepdim=True)
    v = x.var(dim=axis, keepdim=True, unbiased=False)
    return (x - m) / torch.sqrt(v + eps)

def _scatter_add(tgt, idx, src, axis=0):
    return tgt.clone().index_add_(axis, idx, src)

def _segment_sum(src, seg, n):
    out = torch.zeros((n,) + src.shape[1:], dtype=src.dtype)
    return out.index_add_(0, seg, src)

def _pad(x, pw):
    flat = tuple((v for pair in reversed(pw) for v in pair))
    return torch.nn.functional.pad(x, flat)
kernel_name = 'adaLN_cond__gqa_block_cached__mixer_token_mlp__decode'

def setup_inputs():

    _w = lambda a: (lambda t: t.to(torch.bfloat16) if t.is_floating_point() else t)(torch.as_tensor(a))

    def _A_setup():
        B, Sq, D = (2, 1, 1024)
        Hq, Hkv, Sk, Dh = (32, 4, 32768, 128)
        assert Hq % Hkv == 0, (Hq, Hkv)
        return {'x': _w((lambda *s: torch.randn(*s, dtype=torch.bfloat16) * 0.05)(B, Sq, D)), 'k_cache': _w((lambda *s: torch.randn(*s, dtype=torch.bfloat16) * 3.2)(B, Hkv, Sk, Dh)), 'v_cache': _w((lambda *s: torch.randn(*s, dtype=torch.bfloat16) * 1.6)(B, Hkv, Sk, Dh)), 'wq': _w((lambda *s: torch.randn(*s, dtype=torch.bfloat16) * 3.2)(D, Hq * Dh) * D ** (-0.5)), 'wo': _w((lambda *s: torch.randn(*s, dtype=torch.bfloat16) * 1.6)(Hq * Dh, D) * (Hq * Dh) ** (-0.5))}
    _A_in = _A_setup()

    def _B_setup():
        B, S, D, F = (2, 1, 1024, 3584)
        return {'x': _w((lambda *s: torch.randn(*s, dtype=torch.bfloat16) * 0.05)(B, S, D)), 'w1': _w((lambda *s: torch.randn(*s, dtype=torch.bfloat16) * 1.6)(S, F) * S ** (-0.5)), 'b1': _w((lambda *s: torch.randn(*s, dtype=torch.bfloat16) * 1.6)(F) * 0.02), 'w2': _w((lambda *s: torch.randn(*s, dtype=torch.bfloat16) * 1.6)(F, S) * F ** (-0.5)), 'b2': _w((lambda *s: torch.randn(*s, dtype=torch.bfloat16) * 1.6)(S) * 0.02)}
    _B_in = _B_setup()
    return {'A_x': _A_in['x'], 'A_k_cache': _A_in['k_cache'], 'A_v_cache': _A_in['v_cache'], 'A_wq': _A_in['wq'], 'A_wo': _A_in['wo'], 'B_x': _B_in['x'], 'B_w1': _B_in['w1'], 'B_b1': _B_in['b1'], 'B_w2': _B_in['w2'], 'B_b2': _B_in['b2'], 'c': _w((lambda *s: torch.randn(*s, dtype=torch.bfloat16) * 0.05)(2, 1024) * 8.0), 'w_mod': _w((lambda *s: torch.randn(*s, dtype=torch.bfloat16) * 0.05)(1024, 6 * 1024) * 1024 ** (-0.5) * 8.0)}

def reference(A_x, A_k_cache, A_v_cache, A_wq, A_wo, B_x, B_w1, B_b1, B_w2, B_b2, c, w_mod):

    def _A_ref(x, k_cache, v_cache, wq, wo):
        B, Sq, D = x.shape
        Hq, Dh = (32, 128)
        Hkv = k_cache.shape[1]
        rep = Hq // Hkv
        q = (lambda x, *d: x.permute(*d))((x @ wq).reshape(B, Sq, Hq, Dh), 0, 2, 1, 3)
        k = (lambda x, n, axis: torch.repeat_interleave(x, n, dim=axis))(k_cache, rep, axis=1)
        v = (lambda x, n, axis: torch.repeat_interleave(x, n, dim=axis))(v_cache, rep, axis=1)
        scores = torch.einsum('bhqd,bhkd->bhqk', q, k) * Dh ** (-0.5)
        p = _softmax(scores, axis=-1)
        o = torch.einsum('bhqk,bhkd->bhqd', p, v)
        return (lambda x, *d: x.permute(*d))(o, 0, 2, 1, 3).reshape(B, Sq, Hq * Dh) @ wo

    def _B_ref(x, w1, b1, w2, b2):
        B, S, D = x.shape
        h = _layernorm(x, axis=-1)
        u = torch.einsum('bsd,sf->bdf', h, w1) + b1
        u = _gelu(u)
        y = torch.einsum('bdf,fs->bds', u, w2) + b2
        return (lambda x, *d: x.permute(*d))(y, 0, 2, 1)
    x = A_x
    mod = (_silu(c) @ w_mod).reshape(c.shape[0], 6, 1, -1)
    sa, ca, ga, sb, cb, gb = (mod[:, 0].clone(), mod[:, 1].clone(), mod[:, 2].clone(), mod[:, 3].clone(), mod[:, 4].clone(), mod[:, 5].clone())
    h = _layernorm(x, axis=-1) * (1 + ca) + sa
    x = x + ga * _A_ref(h, A_k_cache, A_v_cache, A_wq, A_wo)
    h = _layernorm(x, axis=-1) * (1 + cb) + sb
    x = x + gb * _B_ref(h, B_w1, B_b1, B_w2, B_b2)
    return x
```

```cpp
#include <hip/hip_runtime.h>
#include <hip/hip_bf16.h>
#include <stdint.h>

#define MIMIC 1

typedef unsigned short u16;
typedef unsigned v4u __attribute__((ext_vector_type(4)));
typedef float f32x4 __attribute__((ext_vector_type(4)));
typedef short bf16x8 __attribute__((ext_vector_type(8)));
typedef __bf16 bf16x2_t __attribute__((ext_vector_type(2)));

constexpr int D = 1024, HQ = 32, HKV = 4, SK = 32768, DH = 128, F = 3584, NB = 2;
constexpr int NSPLIT = 32, KEYS_PER_WG = SK / NSPLIT;

__device__ __forceinline__ float bf2f(u16 x) { return __uint_as_float((unsigned)x << 16); }
__device__ __forceinline__ float bflo(unsigned x) { return __uint_as_float(x << 16); }
__device__ __forceinline__ float bfhi(unsigned x) { return __uint_as_float(x & 0xffff0000u); }
__device__ __forceinline__ unsigned cvtpk(float lo, float hi) {
  bf16x2_t v = {(__bf16)lo, (__bf16)hi};
  return __builtin_bit_cast(unsigned, v);
}
__device__ __forceinline__ float rbf(float x) {
  return bflo(cvtpk(x, x) & 0xffffu);
}
__device__ __forceinline__ float mr(float x) { return MIMIC ? rbf(x) : x; }

__device__ __forceinline__ float wave_sum(float v) {
  for (int o = 32; o > 0; o >>= 1) v += __shfl_xor(v, o);
  return v;
}
__device__ __forceinline__ float block_sum256(float v, float* red) {
  v = wave_sum(v);
  __syncthreads();
  if ((threadIdx.x & 63) == 0) red[threadIdx.x >> 6] = v;
  __syncthreads();
  return red[0] + red[1] + red[2] + red[3];
}
__device__ __forceinline__ void ln1024(float (&x)[4], float* red) {
  float s = block_sum256(x[0] + x[1] + x[2] + x[3], red);
  float m = s * (1.f / 1024.f);
  float q = 0.f;
  for (int i = 0; i < 4; ++i) q += (x[i] - m) * (x[i] - m);
  float v = block_sum256(q, red) * (1.f / 1024.f);
  float mb = mr(m), vb = mr(v);
  float sd = mr(sqrtf(mr(vb + 1e-5f)));
  for (int i = 0; i < 4; ++i) x[i] = mr(mr(x[i] - mb) / sd);
}

template <int K>
__device__ __forceinline__ void gemv64(const float* a_lds, const u16* W, int ldw, int n0, float* red  , float* out0, float* out1) {
  const int t = threadIdx.x, c = t & 7, rg = t >> 3;
  float acc[2][8];
  for (int i = 0; i < 8; ++i) acc[0][i] = acc[1][i] = 0.f;
#pragma unroll 8
  for (int k = rg; k < K; k += 32) {
    v4u w = __builtin_nontemporal_load((const v4u*)(W + (size_t)k * ldw + n0 + c * 8));
    float a0 = a_lds[k], a1 = a_lds[K + k];
    for (int i = 0; i < 4; ++i) {
      float lo = bflo(w[i]), hi = bfhi(w[i]);
      acc[0][2 * i] += a0 * lo; acc[0][2 * i + 1] += a0 * hi;
      acc[1][2 * i] += a1 * lo; acc[1][2 * i + 1] += a1 * hi;
    }
  }
  __syncthreads();
  for (int i = 0; i < 8; ++i) { red[(rg * 2 + 0) * 64 + c * 8 + i] = acc[0][i]; red[(rg * 2 + 1) * 64 + c * 8 + i] = acc[1][i]; }
  __syncthreads();
  if (t < 128) {
    int b = t >> 6, n = t & 63;
    float s = 0.f;
    for (int r = 0; r < 32; ++r) s += red[(r * 2 + b) * 64 + n];
    (b ? out1 : out0)[n] = s;
  }
}

__global__ void __launch_bounds__(256) k_mod(const u16* c, const u16* wmod, float* mod) {
  __shared__ float a[2 * D];
  __shared__ float red[32 * 2 * 64];
  __shared__ float o[128];
  for (int i = threadIdx.x; i < 2 * D; i += 256) { float x = bf2f(c[i]); a[i] = mr(x / (1.f + __expf(-x))); }
  __syncthreads();
  int n0 = blockIdx.x * 64;
  gemv64<D>(a, wmod, 6 * D, n0, red, o, o + 64);
  __syncthreads();
  if (threadIdx.x < 128) mod[(threadIdx.x >> 6) * 6 * D + n0 + (threadIdx.x & 63)] = mr(o[threadIdx.x]);
}

__global__ void __launch_bounds__(256) k_q(const u16* x, const float* mod, const u16* wq, u16* q) {
  __shared__ float a[2 * D];
  __shared__ float red[32 * 2 * 64];
  __shared__ float o[128];
  for (int b = 0; b < 2; ++b) {
    float v[4];
    for (int i = 0; i < 4; ++i) v[i] = bf2f(x[b * D + threadIdx.x * 4 + i]);
    ln1024(v, red);
    for (int i = 0; i < 4; ++i) {
      int d = threadIdx.x * 4 + i;
      float sa = mod[b * 6 * D + d], ca = mod[b * 6 * D + D + d];
      a[b * D + d] = mr(mr(v[i] * mr(1.f + ca)) + sa);
    }
  }
  __syncthreads();
  int n0 = blockIdx.x * 64;
  gemv64<D>(a, wq, HQ * DH, n0, red, o, o + 64);
  __syncthreads();
  if (threadIdx.x < 128) q[(threadIdx.x >> 6) * HQ * DH + n0 + (threadIdx.x & 63)] = (u16)(cvtpk(o[threadIdx.x], 0.f) & 0xffff);
}

__device__ __forceinline__ f32x4 mfma16(v4u a, v4u b, f32x4 c) {
  return __builtin_amdgcn_mfma_f32_16x16x32_bf16(__builtin_bit_cast(bf16x8, a), __builtin_bit_cast(bf16x8, b), c, 0, 0, 0);
}
__device__ __forceinline__ float xrow_max(float x) {
  auto s = __builtin_amdgcn_permlane16_swap(__float_as_uint(x), __float_as_uint(x), false, false);
  x = fmaxf(__uint_as_float(s[0]), __uint_as_float(s[1]));
  auto t = __builtin_amdgcn_permlane32_swap(__float_as_uint(x), __float_as_uint(x), false, false);
  return fmaxf(__uint_as_float(t[0]), __uint_as_float(t[1]));
}
__device__ __forceinline__ float xrow_sum(float x) {
  auto s = __builtin_amdgcn_permlane16_swap(__float_as_uint(x), __float_as_uint(x), false, false);
  x = __uint_as_float(s[0]) + __uint_as_float(s[1]);
  auto t = __builtin_amdgcn_permlane32_swap(__float_as_uint(x), __float_as_uint(x), false, false);
  return __uint_as_float(t[0]) + __uint_as_float(t[1]);
}

constexpr int PREC = 132;
__global__ void __launch_bounds__(512) k_attn(const u16* kc, const u16* vc, const u16* q, float* part) {
  __shared__ float mrg[8 * 8 * PREC];
  const int p = blockIdx.x & 7, s = blockIdx.x >> 3;
  const int b = p >> 2, kvh = p & 3;
  const int lane = threadIdx.x & 63, wave = __builtin_amdgcn_readfirstlane(threadIdx.x >> 6);
  const int n = lane & 15, g = lane >> 4;
  const size_t base = ((size_t)p * SK + (size_t)s * KEYS_PER_WG + wave * 128) * DH;
  const u16* kb = kc + base; const u16* vb = vc + base;
  v4u qf[4];
  for (int st = 0; st < 4; ++st) {
    v4u z = {0, 0, 0, 0};
    qf[st] = n < 8 ? *(const v4u*)(q + b * HQ * DH + (kvh * 8 + n) * DH + 32 * st + 8 * g) : z;
  }
  f32x4 acc[8];
  for (int i = 0; i < 8; ++i) acc[i] = f32x4{0, 0, 0, 0};
  float m = -INFINITY, l = 0.f;
  v4u K[2][8], V[2][8];
  auto issue = [&](int t, v4u (&Kx)[8], v4u (&Vx)[8]) {
#pragma unroll
    for (int h = 0; h < 2; ++h)
#pragma unroll
      for (int st = 0; st < 4; ++st)
        Kx[h * 4 + st] = __builtin_nontemporal_load((const v4u*)(kb + (size_t)(t * 32 + h * 16 + n) * DH + 32 * st + 8 * g));
#pragma unroll
    for (int j = 0; j < 8; ++j)
      Vx[j] = __builtin_nontemporal_load((const v4u*)(vb + (size_t)(t * 32 + (j >> 2) * 16 + 4 * g + (j & 3)) * DH + 8 * n));
  };
  auto compute = [&](v4u (&Kx)[8], v4u (&Vx)[8]) {
    f32x4 s0 = {0, 0, 0, 0}, s1 = {0, 0, 0, 0};
#pragma unroll
    for (int st = 0; st < 4; ++st) { s0 = mfma16(Kx[st], qf[st], s0); s1 = mfma16(Kx[4 + st], qf[st], s1); }
    float sc[8];
    constexpr float SCALE = 0.08838834764831845f, LOG2E = 1.4426950408889634f;
#pragma unroll
    for (int j = 0; j < 4; ++j) { sc[j] = mr(mr(s0[j]) * SCALE) * LOG2E; sc[4 + j] = mr(mr(s1[j]) * SCALE) * LOG2E; }
    float mx = sc[0];
#pragma unroll
    for (int j = 1; j < 8; ++j) mx = fmaxf(mx, sc[j]);
    mx = xrow_max(mx);
    float mn = fmaxf(m, mx);
    float alpha = __builtin_amdgcn_exp2f(m - mn);
    m = mn;
    float ps = 0.f; float pv[8];
#pragma unroll
    for (int j = 0; j < 8; ++j) { pv[j] = __builtin_amdgcn_exp2f(sc[j] - mn); ps += pv[j]; }
    l = l * alpha + ps;
    v4u pf = {cvtpk(pv[0], pv[1]), cvtpk(pv[2], pv[3]), cvtpk(pv[4], pv[5]), cvtpk(pv[6], pv[7])};
#pragma unroll
    for (int i = 0; i < 8; ++i) acc[i] *= alpha;
#pragma unroll
    for (int jd = 0; jd < 8; ++jd) {
      v4u a;
#pragma unroll
      for (int pp = 0; pp < 4; ++pp)
        a[pp] = __builtin_amdgcn_perm(Vx[2 * pp + 1][jd >> 1], Vx[2 * pp][jd >> 1], (jd & 1) ? 0x07060302u : 0x05040100u);
      acc[jd] = mfma16(a, pf, acc[jd]);
    }
  };
  issue(0, K[0], V[0]);
  issue(1, K[1], V[1]);
  compute(K[0], V[0]);
  issue(2, K[0], V[0]);
  compute(K[1], V[1]);
  issue(3, K[1], V[1]);
  compute(K[0], V[0]);
  compute(K[1], V[1]);
  l = xrow_sum(l);
  if (n < 8) {
    float* rec = mrg + (wave * 8 + n) * PREC;
#pragma unroll
    for (int r = 0; r < 4; ++r)
#pragma unroll
      for (int jd = 0; jd < 8; ++jd) rec[8 * (4 * g + r) + jd] = acc[jd][r];
    if (g == 0) { rec[128] = m; rec[129] = l; }
  }
  __syncthreads();
  {
    const int h = wave;
    float M = -INFINITY;
    for (int w = 0; w < 8; ++w) M = fmaxf(M, mrg[(w * 8 + h) * PREC + 128]);
    float L = 0.f, o0 = 0.f, o1 = 0.f;
    for (int w = 0; w < 8; ++w) {
      const float* rec = mrg + (w * 8 + h) * PREC;
      float wt = __builtin_amdgcn_exp2f(rec[128] - M);
      L += wt * rec[129]; o0 += wt * rec[2 * lane]; o1 += wt * rec[2 * lane + 1];
    }
    float* dst = part + ((size_t)(p * NSPLIT + s) * 8 + h) * PREC;
    dst[2 * lane] = o0; dst[2 * lane + 1] = o1;
    if (lane == 0) { dst[128] = M; dst[129] = L; }
  }
}

__global__ void __launch_bounds__(128) k_comb(const float* part, float* o) {
  const int b = blockIdx.x >> 5, hq = blockIdx.x & 31, p = b * 4 + (hq >> 3), h = hq & 7;
  const float* src = part + ((size_t)p * NSPLIT * 8 + h) * PREC;
  float M = -INFINITY;
  for (int s = 0; s < NSPLIT; ++s) M = fmaxf(M, src[(size_t)s * 8 * PREC + 128]);
  float L = 0.f, acc = 0.f;
  for (int s = 0; s < NSPLIT; ++s) {
    const float* rec = src + (size_t)s * 8 * PREC;
    float wt = __builtin_amdgcn_exp2f(rec[128] - M);
    L += wt * rec[129]; acc += wt * rec[threadIdx.x];
  }
  o[b * HQ * DH + hq * DH + threadIdx.x] = mr(acc / L);
}

__global__ void __launch_bounds__(256) k_wo(const float* o, const u16* wo, float* attn_part) {
  __shared__ float a[2 * 512];
  __shared__ float red[32 * 2 * 64];
  __shared__ float r[128];
  const int n0 = blockIdx.x * 64, k0 = blockIdx.y * 512;
  for (int i = threadIdx.x; i < 1024; i += 256) a[i] = o[(i >> 9) * HQ * DH + k0 + (i & 511)];
  __syncthreads();
  gemv64<512>(a, wo + (size_t)k0 * D, D, n0, red, r, r + 64);
  __syncthreads();
  if (threadIdx.x < 128) attn_part[(size_t)blockIdx.y * 2 * D + (threadIdx.x >> 6) * D + n0 + (threadIdx.x & 63)] = r[threadIdx.x];
}

__global__ void __launch_bounds__(256) k_fin(const u16* x, const float* mod, const float* attn_part, const u16* w1, const u16* b1,
                                             const u16* w2, const u16* b2, u16* out) {
  __shared__ float red[8];
  __shared__ float h2s[8], x1s[8];
  __shared__ float part[4][8];
  const int b = blockIdx.x >> 7, d0 = (blockIdx.x & 127) * 8;
  const float* mb = mod + b * 6 * D;
  float v[4], x1[4];
  for (int i = 0; i < 4; ++i) {
    int d = threadIdx.x * 4 + i;
    float a = 0.f;
    for (int k = 0; k < 8; ++k) a += attn_part[(size_t)k * 2 * D + b * D + d];
    a = mr(a);
    x1[i] = mr(bf2f(x[b * D + d]) + mr(mb[2 * D + d] * a));
    v[i] = x1[i];
  }
  ln1024(v, red);
  for (int i = 0; i < 4; ++i) {
    int d = threadIdx.x * 4 + i;
    v[i] = mr(mr(v[i] * mr(1.f + mb[4 * D + d])) + mb[3 * D + d]);
  }
  ln1024(v, red);
  for (int i = 0; i < 4; ++i) {
    int d = threadIdx.x * 4 + i;
    if (d >= d0 && d < d0 + 8) { h2s[d - d0] = v[i]; x1s[d - d0] = x1[i]; }
  }
  __syncthreads();
  float acc[8];
  for (int i = 0; i < 8; ++i) acc[i] = 0.f;
  for (int f = threadIdx.x; f < F; f += 256) {
    float a1 = bf2f(w1[f]), c1 = bf2f(b1[f]), a2 = bf2f(w2[f]);
    for (int i = 0; i < 8; ++i) {
      float u = h2s[i] * a1 + c1;
      float z = u * (-2.302208198f - 0.1029432397f * u * u);
      float ge = u * __builtin_amdgcn_rcpf(1.f + __builtin_amdgcn_exp2f(z));
      acc[i] += ge * a2;
    }
  }
  for (int i = 0; i < 8; ++i) { float s = wave_sum(acc[i]); if ((threadIdx.x & 63) == 0) part[threadIdx.x >> 6][i] = s; }
  __syncthreads();
  if (threadIdx.x < 8) {
    int i = threadIdx.x, d = d0 + i;
    float y = mr(mr(part[0][i] + part[1][i] + part[2][i] + part[3][i]) + bf2f(b2[0]));
    float r = x1s[i] + mr(mb[5 * D + d] * y);
    out[b * D + d] = (u16)(cvtpk(r, 0.f) & 0xffff);
  }
}

extern "C" void kernel_launch(void* const* d_in, const int* in_sizes, int n_in, void* d_out, int out_size, void* d_ws, size_t ws_size,
                              hipStream_t stream) {
  const u16* x = (const u16*)d_in[0];
  const u16* kc = (const u16*)d_in[1];
  const u16* vc = (const u16*)d_in[2];
  const u16* wq = (const u16*)d_in[3];
  const u16* wo = (const u16*)d_in[4];
  const u16* w1 = (const u16*)d_in[6];
  const u16* b1 = (const u16*)d_in[7];
  const u16* w2 = (const u16*)d_in[8];
  const u16* b2 = (const u16*)d_in[9];
  const u16* c = (const u16*)d_in[10];
  const u16* wmod = (const u16*)d_in[11];
  char* ws = (char*)d_ws;
  float* mod = (float*)ws;
  u16* q = (u16*)(ws + 65536);
  float* o = (float*)(ws + 98304);
  float* ap = (float*)(ws + 131072);
  float* part = (float*)(ws + 262144);
  k_mod<<<96, 256, 0, stream>>>(c, wmod, mod);
  k_q<<<64, 256, 0, stream>>>(x, mod, wq, q);
  k_attn<<<256, 512, 0, stream>>>(kc, vc, q, part);
  k_comb<<<64, 128, 0, stream>>>(part, o);
  k_wo<<<dim3(16, 8), 256, 0, stream>>>(o, wo, ap);
  k_fin<<<256, 256, 0, stream>>>(x, mod, ap, w1, b1, w2, b2, (u16*)d_out);
}
```

```cpp
#include <hip/hip_runtime.h>
#include <stdint.h>

#define STAMP 0
#define WIN 24
#define PAD 14

namespace {

typedef unsigned short u16;
typedef unsigned long long u64;
typedef unsigned v4u __attribute__((ext_vector_type(4)));
typedef unsigned v2u __attribute__((ext_vector_type(2)));
typedef float f32x4 __attribute__((ext_vector_type(4)));
typedef float f32x2 __attribute__((ext_vector_type(2)));
typedef short bf16x8 __attribute__((ext_vector_type(8)));
typedef short s16x4 __attribute__((ext_vector_type(4)));
typedef __bf16 bf16x2_t __attribute__((ext_vector_type(2)));
#define LAS __attribute__((address_space(3)))

constexpr int D = 1024, SK = 32768, DH = 128, F = 3584;
constexpr unsigned SENT = 0xFFFFFFFFu;
constexpr int AUX_SC1 = 16;
constexpr unsigned GCAP = 1u << 17, LCAP = 1u << 22;

constexpr int WS_X = 0;
constexpr int WS_RDY = 4096;
constexpr int WS_P1 = 16384;
constexpr int WS_CLR = WS_P1 + 131072;
constexpr int WS_QP = WS_CLR;
constexpr int WS_MP = WS_QP + 262144;
constexpr int WS_OR = WS_MP + 262144;
constexpr int WS_AP = WS_OR + 524288;
constexpr int WS_MR = WS_AP + 65536;
constexpr int WS_ST = WS_MR + 16384;
constexpr int WS_TB = WS_ST + 16384;
constexpr int WS_END = WS_TB + 16384;
constexpr int CLR_PER_WG = (WS_END - WS_CLR) / 256;
static_assert(CLR_PER_WG == 4544, "clear share");
constexpr int WS_STAMP = 2 * 1024 * 1024;

constexpr int L_RING = 0;
constexpr int L_WQ = 16 * 4096, L_MRS = 24 * 4096;
constexpr int L_P = 131072;
constexpr int L_CTRL = L_P + 16384;
constexpr int C_FULL = 0, C_RDY = 1, C_PROG = 8, C_BAR = 16, C_DUMMY = 64;
constexpr int L_SV = L_CTRL + 512, L_HV = L_SV + 512, L_QV = L_HV + 512;
constexpr int L_MX = L_QV + 2176, L_LS = L_MX + 256, L_LNS = L_LS + 256, L_TBP = L_LNS + 64;
constexpr int L_LNR = L_TBP + 640, L_T1R = L_LNR + 128;
constexpr int L_BYTES = L_T1R + 4096;
static_assert(L_BYTES <= 163840 && (L_T1R % 16) == 0 && (L_QV % 16) == 0 && (L_MX % 16) == 0, "LDS map");

constexpr int G_S1 = 0, G_WQ = 4, G_MR = 12, G_K = 20, G_V = 84, G_WO = 148, G_END = 156;

__device__ __forceinline__ float bflo(unsigned x) { return __uint_as_float(x << 16); }
__device__ __forceinline__ float bfhi(unsigned x) { return __uint_as_float(x & 0xffff0000u); }
__device__ __forceinline__ unsigned cvtpk(float lo, float hi) {
  bf16x2_t v = {(__bf16)lo, (__bf16)hi};
  return __builtin_bit_cast(unsigned, v);
}
__device__ __forceinline__ float rbf(float x) { return bflo(cvtpk(x, x)); }
__device__ __forceinline__ unsigned nosent(unsigned u) { return u < 0xFFFFFFFEu ? u : 0xFFFFFFFEu; }
__device__ __forceinline__ v4u nosent4(f32x4 v) {
  return v4u{nosent(__float_as_uint(v[0])), nosent(__float_as_uint(v[1])), nosent(__float_as_uint(v[2])), nosent(__float_as_uint(v[3]))};
}
__device__ __forceinline__ unsigned umax4(v4u v) { return max(max(v[0], v[1]), max(v[2], v[3])); }
__device__ __forceinline__ f32x4 mfma16(v4u a, v4u b, f32x4 c) {
  return __builtin_amdgcn_mfma_f32_16x16x32_bf16(__builtin_bit_cast(bf16x8, a), __builtin_bit_cast(bf16x8, b), c, 0, 0, 0);
}
template <int CTRL> __device__ __forceinline__ float dpp(float x) {
  return __builtin_bit_cast(float, __builtin_amdgcn_mov_dpp(__builtin_bit_cast(int, x), CTRL, 0xf, 0xf, true));
}
constexpr int XOR1 = 0xB1, XOR2 = 0x4E, XOR7 = 0x141, ROR8 = 0x128;
__device__ __forceinline__ float swap32_add(float a, float b) {
  auto s = __builtin_amdgcn_permlane32_swap(__float_as_uint(a), __float_as_uint(b), false, false);
  return __uint_as_float(s[0]) + __uint_as_float(s[1]);
}
__device__ __forceinline__ float swap16_add(float a, float b) {
  auto s = __builtin_amdgcn_permlane16_swap(__float_as_uint(a), __float_as_uint(b), false, false);
  return __uint_as_float(s[0]) + __uint_as_float(s[1]);
}
__device__ __forceinline__ float xrow_sum(float x) { x = swap16_add(x, x); return swap32_add(x, x); }
__device__ __forceinline__ float xrow_max(float x) {
  auto s = __builtin_amdgcn_permlane16_swap(__float_as_uint(x), __float_as_uint(x), false, false);
  x = fmaxf(__uint_as_float(s[0]), __uint_as_float(s[1]));
  auto t = __builtin_amdgcn_permlane32_swap(__float_as_uint(x), __float_as_uint(x), false, false);
  return fmaxf(__uint_as_float(t[0]), __uint_as_float(t[1]));
}
__device__ __forceinline__ float wave_sum(float x) {
  x += dpp<XOR1>(x); x += dpp<XOR2>(x); x += dpp<XOR7>(x); x += dpp<ROR8>(x);
  return xrow_sum(x);
}
__device__ __forceinline__ u64 rt() { return __builtin_amdgcn_s_memrealtime(); }

__device__ __forceinline__ v2u trread(LAS unsigned char* p) {
  return __builtin_bit_cast(v2u, __builtin_amdgcn_ds_read_tr16_b64_v4i16((LAS s16x4*)p));
}
__device__ __forceinline__ v4u trfrag(LAS unsigned char* p, int step) {
  const v2u a = trread(p), b = trread(p + step);
  return v4u{a[0], a[1], b[0], b[1]};
}
#define MEMBAR() asm volatile("" ::: "memory")

__device__ __forceinline__ void lds_wait_ge(volatile LAS unsigned* w, unsigned need) {
  _Pragma("nounroll") for (unsigned i = 0; i < LCAP; ++i) {
    if ((unsigned)__builtin_amdgcn_readfirstlane(*w) >= need) break;
    __builtin_amdgcn_s_sleep(1);
  }
  MEMBAR();
}
__device__ __forceinline__ void lds_arrive(LAS unsigned* w, int lane) {
  asm volatile("s_waitcnt lgkmcnt(0)" ::: "memory");
  if (lane == 0) __hip_atomic_fetch_add(w, 1u, __ATOMIC_RELAXED, __HIP_MEMORY_SCOPE_WORKGROUP);
  MEMBAR();
}

template <int W, int P>
__device__ __forceinline__ void dma_piece(const void* sbase, unsigned voff, unsigned ldsaddr) {
  asm volatile("s_mov_b32 m0, %2\n\ts_nop 0\n\tglobal_load_lds_dwordx4 %1, %0 nt\n\ts_waitcnt vmcnt(%3)\n\t.rept %4\n\ts_nop 0\n\t.endr"
               :: "s"(sbase), "v"(voff), "s"(ldsaddr), "i"(W), "i"(P) : "memory");
}
template <int W, int P>
__device__ __forceinline__ void dma_group(const void* sbase, unsigned v0, unsigned v1, unsigned v2, unsigned v3, unsigned ldsaddr) {
  asm volatile("s_mov_b32 m0, %5\n\ts_nop 0\n\t"
               "global_load_lds_dwordx4 %1, %0 nt\n\ts_waitcnt vmcnt(%6)\n\t.rept %7\n\ts_nop 0\n\t.endr\n\t"
               "global_load_lds_dwordx4 %2, %0 offset:1024 nt\n\ts_waitcnt vmcnt(%6)\n\t.rept %7\n\ts_nop 0\n\t.endr\n\t"
               "global_load_lds_dwordx4 %3, %0 offset:2048 nt\n\ts_waitcnt vmcnt(%6)\n\t.rept %7\n\ts_nop 0\n\t.endr\n\t"
               "global_load_lds_dwordx4 %4, %0 offset:3072 nt\n\ts_waitcnt vmcnt(%6)\n\t.rept %7 - 2\n\ts_nop 0\n\t.endr"
               :: "s"(sbase), "v"(v0), "v"(v1), "v"(v2), "v"(v3), "s"(ldsaddr), "i"(W), "i"(P) : "memory");
}

__device__ __forceinline__ float gelu_term(float h, float w1, float b1, float w2, float acc) {
  const float u = fmaf(h, w1, b1);
  const float z = u * fmaf(u * u, -0.1029432397f, -2.302208198f);
  return fmaf(u * __builtin_amdgcn_rcpf(1.f + __builtin_amdgcn_exp2f(z)), w2, acc);
}

#define ST(i) do {} while (0)

__global__ void __launch_bounds__(576, 3)
fused_block(const u16* __restrict__ xin, const u16* __restrict__ kc, const u16* __restrict__ vc, const u16* __restrict__ wq,
            const u16* __restrict__ wo, const u16* __restrict__ w1, const u16* __restrict__ b1, const u16* __restrict__ w2,
            const u16* __restrict__ b2, const u16* __restrict__ cin, const u16* __restrict__ wmod, u16* __restrict__ out,
            unsigned char* ws) {
  extern __shared__ __attribute__((aligned(16))) unsigned char lds_raw[];
  LAS unsigned char* L = (LAS unsigned char*)lds_raw;
  volatile LAS unsigned* ctrl = (volatile LAS unsigned*)(L + L_CTRL);
  const int j = blockIdx.x, x = j & 7, r = j >> 3;
  const int lane = threadIdx.x & 63, w = __builtin_amdgcn_readfirstlane(threadIdx.x >> 6);
  const int ks = r & 7, combo = r >> 3;
  const int kvh_t = x >> 1, u_t = (x & 1) * 2 + (r >> 4), cg_t = r & 15;
  ST(0);

  if (w == 8) {
    const unsigned l0 = (unsigned)(size_t)L;
    ctrl[lane] = 0u;
    asm volatile("s_waitcnt lgkmcnt(0)" ::: "memory");
    volatile LAS unsigned* fw = ctrl + (lane == 0 ? C_FULL : C_DUMMY + lane);
    const int q4 = lane >> 4, c16 = lane & 15, r8 = lane >> 3, c8 = lane & 7;
    {
      const unsigned vo = r8 * 12288 + ((c8 ^ (2 * ((r8 >> 1) & 3))) << 4);
      const char* sb = (const char*)wmod + (size_t)(ks * 128) * 12288 + ((combo >> 1) * 1024 + x * 128 + (combo & 1) * 64) * 2;
      _Pragma("nounroll") for (int i = 0; i < 16; ++i) dma_piece<WIN, 0>(sb + (size_t)i * (8 * 12288), vo, l0 + L_P + i * 1024);
    }
    __builtin_amdgcn_s_barrier();
    const unsigned sw0 = (c16 ^ (2 * q4)) << 4, sw1 = (c16 ^ (2 * (4 + q4))) << 4;
    int G = G_WQ;
    {
      const unsigned va = q4 * 8192 + sw0, vb = q4 * 8192 + sw1;
      const char* sb = (const char*)wq + (size_t)(x * 128) * 8192 + r * 256;
      _Pragma("nounroll") for (int i = 0; i < 8; ++i, ++G) {
        for (int k = 0; k < 4; ++k) dma_piece<WIN, 0>(sb + (size_t)(i * 4 + k) * (4 * 8192), (k & 1) ? vb : va, l0 + L_WQ + (i * 4 + k) * 1024);
        if (G + 1 > WIN / 4) *fw = G + 1 - WIN / 4;
      }
    }
    {
      const unsigned va = q4 * 12288 + sw0, vb = q4 * 12288 + sw1;
      const char* sb = (const char*)wmod + (size_t)(ks * 128) * 12288 + 4096 + (x + 8 * combo) * 256;
      _Pragma("nounroll") for (int i = 0; i < 8; ++i, ++G) {
        for (int k = 0; k < 4; ++k) dma_piece<WIN, 0>(sb + (size_t)(i * 4 + k) * (4 * 12288), (k & 1) ? vb : va, l0 + L_MRS + (i * 4 + k) * 1024);
        *fw = G + 1 - WIN / 4;
      }
    }
    ST(1);
    unsigned freed = 0;
    auto need_slot = [&](int ri) {
      if (ri >= 16 && freed < (unsigned)(ri - 15)) {
        _Pragma("nounroll") for (unsigned i = 0; i < LCAP; ++i) {
          unsigned pv = ctrl[C_PROG + (lane & 7)];
          pv = min(pv, (unsigned)__builtin_amdgcn_mov_dpp((int)pv, XOR1, 0xf, 0xf, true));
          pv = min(pv, (unsigned)__builtin_amdgcn_mov_dpp((int)pv, XOR2, 0xf, 0xf, true));
          pv = min(pv, (unsigned)__builtin_amdgcn_mov_dpp((int)pv, XOR7, 0xf, 0xf, true));
          freed = __builtin_amdgcn_readfirstlane(pv);
          if (freed >= (unsigned)(ri - 15)) break;
          asm volatile("s_waitcnt vmcnt(0)" ::: "memory");
          *fw = G;
        }
      }
    };
    const size_t kvbase = ((size_t)x * SK + (size_t)r * 1024) * 256;
    {
      const unsigned k0 = q4 * 256 + ((c16 ^ q4) << 4), k1 = q4 * 256 + ((c16 ^ (4 + q4)) << 4),
                     k2 = q4 * 256 + ((c16 ^ (8 + q4)) << 4), k3 = q4 * 256 + ((c16 ^ (12 + q4)) << 4);
      const char* sb = (const char*)kc + kvbase;
      unsigned hi = G - WIN / 4;
      _Pragma("nounroll") for (int i = 0; i < 64; ++i, ++G) {
        const unsigned before = freed;
        need_slot(i);
        if (freed != before) hi = max(hi, (unsigned)__builtin_amdgcn_readfirstlane(ctrl[C_FULL]));
        dma_group<WIN, PAD>(sb + (size_t)i * 4096, k0, k1, k2, k3, l0 + (i & 31) * 4096);
        hi = max(hi, (unsigned)(G + 1 - WIN / 4));
        *fw = hi;
      }
      ST(2);
      const unsigned v0 = q4 * 256 + sw0, v1 = q4 * 256 + sw1;
      sb = (const char*)vc + kvbase;
      _Pragma("nounroll") for (int i = 0; i < 64; ++i, ++G) {
        const unsigned before = freed;
        need_slot(64 + i);
        if (freed != before) hi = max(hi, (unsigned)__builtin_amdgcn_readfirstlane(ctrl[C_FULL]));
        dma_group<WIN, PAD>(sb + (size_t)i * 4096, v0, v1, v0, v1, l0 + (i & 31) * 4096);
        hi = max(hi, (unsigned)(G + 1 - WIN / 4));
        *fw = hi;
      }
      ST(3);
      const unsigned rowoff = (lane >> 5) * (128 * 2048) + (r8 & 3) * 2048;
      const unsigned wa = rowoff + ((c8 ^ (2 * ((r8 >> 1) & 1))) << 4), wb = rowoff + ((c8 ^ (2 * (((r8 >> 1) & 1) | 2))) << 4);
      sb = (const char*)wo + (size_t)(kvh_t * 1024 + 32 * u_t) * 2048 + cg_t * 128;
      _Pragma("nounroll") for (int i = 0; i < 8; ++i, ++G) {
        const unsigned before = freed;
        need_slot(128 + i);
        if (freed != before) hi = max(hi, (unsigned)__builtin_amdgcn_readfirstlane(ctrl[C_FULL]));
        for (int k = 0; k < 4; ++k)
          dma_piece<WIN, PAD>(sb + (size_t)(k * 256 + 4 * i) * 2048, (k & 1) ? wb : wa, l0 + i * 4096 + k * 1024);
        hi = max(hi, (unsigned)(G + 1 - WIN / 4));
        *fw = hi;
      }
#define DRAIN(n) if (WIN > (n)) { asm volatile("s_waitcnt vmcnt(" #n ")" ::: "memory"); hi = max(hi, (unsigned)(G_END - (n) / 4)); *fw = hi; }
      DRAIN(44) DRAIN(40) DRAIN(36) DRAIN(32) DRAIN(28) DRAIN(24) DRAIN(20) DRAIN(16) DRAIN(12) DRAIN(8) DRAIN(4) DRAIN(0)
#undef DRAIN
    }
    ST(4);
    return;
  }

  const auto wsr = __builtin_amdgcn_make_buffer_rsrc(ws, 0, WS_END, 0x00020000);
  const int n = lane & 15, g = lane >> 4, tq = (lane >> 2) & 3, tp = lane & 3;
  LAS unsigned* bar = (LAS unsigned*)(L + L_CTRL) + C_BAR;
  volatile LAS unsigned* vbar = ctrl + C_BAR;
  volatile LAS unsigned* fullw = ctrl + C_FULL;
  const int tid = threadIdx.x;

  const int hb = (lane >> 2) & 1;
  const v2u xh = *(const v2u*)(xin + hb * D + 128 * x + 16 * w + 4 * tp);
  unsigned tag = 0;
  if (w == 7) {
    const v4u s4 = {SENT, SENT, SENT, SENT};
    const unsigned X = __builtin_amdgcn_raw_buffer_load_b32(wsr, WS_X, 0, AUX_SC1);
    if (lane < 32)
      __builtin_amdgcn_raw_buffer_store_b128(s4, wsr, WS_P1 + ((x * 8 + ks) * 2 + (combo >> 1)) * 1024 + (lane >> 4) * 512 + (combo & 1) * 256 + (lane & 15) * 16, 0, AUX_SC1);
    const int cb = WS_CLR + j * CLR_PER_WG + lane * 16;
#pragma unroll
    for (int i = 0; i < 4; ++i) __builtin_amdgcn_raw_buffer_store_b128(s4, wsr, cb + i * 1024, 0, AUX_SC1);
    if (lane < 28) __builtin_amdgcn_raw_buffer_store_b128(s4, wsr, cb + 4096, 0, AUX_SC1);
    __builtin_amdgcn_s_barrier();
    asm volatile("s_waitcnt vmcnt(0)" ::: "memory");
    tag = X * 1664525u + 1013904223u;
    if (lane == 0) __builtin_amdgcn_raw_buffer_store_b32(tag, wsr, WS_RDY + j * 32, 0, AUX_SC1);
  } else if (w == 6) {
    const v2u cv = *(const v2u*)(cin + (lane >> 5) * D + 128 * ks + 4 * (lane & 31));
    __builtin_amdgcn_s_barrier();
    float s[4] = {bflo(cv[0]), bfhi(cv[0]), bflo(cv[1]), bfhi(cv[1])};
#pragma unroll
    for (int i = 0; i < 4; ++i) s[i] = s[i] / (1.f + __expf(-s[i]));
    const int kq = lane & 31;
    *(LAS v2u*)(L + L_SV + (lane >> 5) * 256 + (kq >> 3) * 64 + (kq & 3) * 16 + ((kq >> 2) & 1) * 8) = v2u{cvtpk(s[0], s[1]), cvtpk(s[2], s[3])};
  } else if (w == 4 || w == 5) {
    const v4u xa = *(const v4u*)(xin + (w - 4) * D + lane * 16), xb = *(const v4u*)(xin + (w - 4) * D + lane * 16 + 8);
    __builtin_amdgcn_s_barrier();
    float v[16];
#pragma unroll
    for (int i = 0; i < 4; ++i) { v[2 * i] = bflo(xa[i]); v[2 * i + 1] = bfhi(xa[i]); v[8 + 2 * i] = bflo(xb[i]); v[9 + 2 * i] = bfhi(xb[i]); }
    float s = 0.f;
#pragma unroll
    for (int i = 0; i < 16; ++i) s += v[i];
    const float m = wave_sum(s) * (1.f / 1024.f);
    float q = 0.f;
#pragma unroll
    for (int i = 0; i < 16; ++i) q = fmaf(v[i] - m, v[i] - m, q);
    const float var = wave_sum(q) * (1.f / 1024.f);
    if (lane == 0) *(LAS f32x2*)(L + L_LNS + (w - 4) * 8) = f32x2{rbf(m), rbf(sqrtf(rbf(rbf(var) + 1e-5f)))};
  } else {
    __builtin_amdgcn_s_barrier();
  }
  lds_arrive(bar + 0, lane);
  ST(1);

  float mw1[7], mb1[7], mw2[7];
#pragma unroll
  for (int i = 0; i < 7; ++i) { mw1[i] = bflo(w1[tid + 512 * i]); mb1[i] = bflo(b1[tid + 512 * i]); mw2[i] = bflo(w2[tid + 512 * i]); }

  lds_wait_ge(vbar + 0, 8);
  v4u sf[4];
#pragma unroll
  for (int st = 0; st < 4; ++st) sf[st] = *(LAS v4u*)(L + L_SV + (n & 1) * 256 + st * 64 + g * 16);
  if (w < 4) {
    lds_wait_ge(fullw, G_S1 + 4);
    LAS unsigned char* a = L + L_P + (4 * g + tq) * 128 + (((2 * w + (tp >> 1)) ^ (2 * (2 * (g & 1) + (tq >> 1)))) << 4) + (tp & 1) * 8;
    f32x4 acc = {0.f, 0.f, 0.f, 0.f};
#pragma unroll
    for (int st = 0; st < 4; ++st) acc = mfma16(trfrag(a + st * 4096, 2048), sf[st], acc);
    if (n < 2)
      __builtin_amdgcn_raw_buffer_store_b128(nosent4(acc), wsr, WS_P1 + ((x * 8 + ks) * 2 + (combo >> 1)) * 1024 + n * 512 + ((combo & 1) * 64 + 16 * w + 4 * g) * 4, 0, AUX_SC1);
  }
  ST(2);
  if (w == 7) {
    _Pragma("nounroll") for (unsigned i = 0; i < GCAP; ++i) {
      MEMBAR();
      bool ok = true;
#pragma unroll
      for (int k = 0; k < 4; ++k) ok &= __builtin_amdgcn_raw_buffer_load_b32(wsr, WS_RDY + (k * 64 + lane) * 32, 0, AUX_SC1) == tag;
      if (__all(ok)) break;
      __builtin_amdgcn_s_sleep(8);
    }
    if (lane == 0) ctrl[C_RDY] = 1u;
    if (j == 0 && lane == 0) __builtin_amdgcn_raw_buffer_store_b32(tag, wsr, WS_X, 0, AUX_SC1);
  }
  const int vtr = (4 * g + tq) * 256 + (((2 * w + (tp >> 1)) ^ (2 * (4 * (g & 1) + tq))) << 4) + (tp & 1) * 8;
  v4u wqf[4];
  lds_wait_ge(fullw, G_WQ + 8);
#pragma unroll
  for (int st = 0; st < 4; ++st) wqf[st] = trfrag(L + L_WQ + vtr + st * 8192, 4096);
  asm volatile("s_waitcnt lgkmcnt(0)" ::: "memory");
  if (lane == 0) ctrl[C_PROG + w] = 8u;
  lds_wait_ge(ctrl + C_RDY, 1);
  ST(3);

  {
    const int o1 = WS_P1 + (x * 8 + (lane >> 3)) * 2048 + hb * 512 + (16 * w + 4 * tp) * 4;
    v4u pa, pc;
    _Pragma("nounroll") for (unsigned i = 0; i < GCAP; ++i) {
      MEMBAR();
      pa = __builtin_amdgcn_raw_buffer_load_b128(wsr, o1, 0, AUX_SC1);
      pc = __builtin_amdgcn_raw_buffer_load_b128(wsr, o1 + 1024, 0, AUX_SC1);
      if (__all(max(umax4(pa), umax4(pc)) != SENT)) break;
      __builtin_amdgcn_s_sleep(4);
    }
    const f32x2 ln = *(LAS f32x2*)(L + L_LNS + hb * 8);
    const float xv[4] = {bflo(xh[0]), bfhi(xh[0]), bflo(xh[1]), bfhi(xh[1])};
    float hv[4];
#pragma unroll
    for (int i = 0; i < 4; ++i) {
      float sa = __uint_as_float(pa[i]), ca = __uint_as_float(pc[i]);
      sa += dpp<ROR8>(sa); ca += dpp<ROR8>(ca);
      sa = rbf(xrow_sum(sa)); ca = rbf(xrow_sum(ca));
      const float nv = rbf(rbf(xv[i] - ln[0]) / ln[1]);
      hv[i] = rbf(nv * rbf(1.f + ca)) + sa;
    }
    if (lane < 8) *(LAS v2u*)(L + L_HV + hb * 256 + (w >> 1) * 64 + tp * 16 + (w & 1) * 8) = v2u{cvtpk(hv[0], hv[1]), cvtpk(hv[2], hv[3])};
  }
  ST(4);
  lds_arrive(bar + 1, lane);
  lds_wait_ge(vbar + 1, 8);
  {
    f32x4 acc = {0.f, 0.f, 0.f, 0.f};
#pragma unroll
    for (int st = 0; st < 4; ++st) acc = mfma16(wqf[st], *(LAS v4u*)(L + L_HV + (n & 1) * 256 + st * 64 + g * 16), acc);
    if (n < 2)
      __builtin_amdgcn_raw_buffer_store_b128(nosent4(acc), wsr, WS_QP + ((n * 4 + (r >> 3)) * 8 + x) * 4096 + ((r & 7) * 128 + 16 * w + 4 * g) * 4, 0, AUX_SC1);
  }
  ST(5);
  {
    lds_wait_ge(fullw, G_MR + 8);
    f32x4 acc = {0.f, 0.f, 0.f, 0.f};
#pragma unroll
    for (int st = 0; st < 4; ++st) acc = mfma16(trfrag(L + L_MRS + vtr + st * 8192, 4096), sf[st], acc);
    if (n < 2)
      __builtin_amdgcn_raw_buffer_store_b128(nosent4(acc), wsr, WS_MP + ((x + 8 * combo) * 8 + ks) * 1024 + n * 512 + (16 * w + 4 * g) * 4, 0, AUX_SC1);
  }
  asm volatile("s_waitcnt lgkmcnt(0)" ::: "memory");
  if (lane == 0) ctrl[C_PROG + w] = 16u + 2 * w;

  int kofs[4];
#pragma unroll
  for (int st = 0; st < 4; ++st) kofs[st] = n * 256 + (((4 * st + g) ^ n) << 4);
  v4u kf[8];
  auto kread = [&](int t) {
    lds_wait_ge(fullw, G_K + 2 * t + 2);
    LAS unsigned char* kb = L + ((2 * t) & 31) * 4096;
#pragma unroll
    for (int st = 0; st < 4; ++st) { kf[st] = *(LAS v4u*)(kb + kofs[st]); kf[4 + st] = *(LAS v4u*)(kb + 4096 + kofs[st]); }
    asm volatile("s_waitcnt lgkmcnt(0)" ::: "memory");
    if (lane == 0) ctrl[C_PROG + w] = 16u + (t < 24 ? 2 * (t + 8) : 64);
  };
  kread(w);
  ST(6);
  {
    const int o2 = WS_QP + (x * 8 + (lane >> 5)) * 4096 + (w * 128 + 4 * (lane & 31)) * 4;
    v4u pq[4];
    _Pragma("nounroll") for (unsigned i = 0; i < GCAP; ++i) {
      MEMBAR();
#pragma unroll
      for (int k = 0; k < 4; ++k) pq[k] = __builtin_amdgcn_raw_buffer_load_b128(wsr, o2 + k * 8192, 0, AUX_SC1);
      if (__all(max(max(umax4(pq[0]), umax4(pq[1])), max(umax4(pq[2]), umax4(pq[3]))) != SENT)) break;
      __builtin_amdgcn_s_sleep(4);
    }
    float qv[4];
#pragma unroll
    for (int i = 0; i < 4; ++i) {
      const float s = (__uint_as_float(pq[0][i]) + __uint_as_float(pq[1][i])) + (__uint_as_float(pq[2][i]) + __uint_as_float(pq[3][i]));
      qv[i] = swap32_add(s, s);
    }
    if (lane < 32) *(LAS v2u*)(L + L_QV + w * 272 + lane * 8) = v2u{cvtpk(qv[0], qv[1]), cvtpk(qv[2], qv[3])};
  }
  ST(7);
  lds_arrive(bar + 2, lane);
  lds_wait_ge(vbar + 2, 8);
  v4u qf[4];
#pragma unroll
  for (int st = 0; st < 4; ++st) qf[st] = *(LAS v4u*)(L + L_QV + (n & 7) * 272 + st * 64 + g * 16);
  float sc[32];
#pragma unroll
  for (int i = 0; i < 4; ++i) {
    if (i > 0) kread(w + 8 * i);
    f32x4 s0 = {0.f, 0.f, 0.f, 0.f}, s1 = {0.f, 0.f, 0.f, 0.f};
#pragma unroll
    for (int st = 0; st < 4; ++st) { s0 = mfma16(kf[st], qf[st], s0); s1 = mfma16(kf[4 + st], qf[st], s1); }
    constexpr float SCALE = 0.08838834764831845f, LOG2E = 1.4426950408889634f;
#pragma unroll
    for (int k = 0; k < 4; ++k) { sc[8 * i + k] = rbf(rbf(s0[k]) * SCALE) * LOG2E; sc[8 * i + 4 + k] = rbf(rbf(s1[k]) * SCALE) * LOG2E; }
  }
  ST(8);
  float mx = sc[0];
#pragma unroll
  for (int i = 1; i < 32; ++i) mx = fmaxf(mx, sc[i]);
  mx = xrow_max(mx);
  if (lane < 8) *(LAS float*)(L + L_MX + lane * 32 + w * 4) = mx;
  lds_arrive(bar + 3, lane);
  lds_wait_ge(vbar + 3, 8);
  float Mw;
  {
    const f32x4 a = *(LAS f32x4*)(L + L_MX + (n & 7) * 32), b = *(LAS f32x4*)(L + L_MX + (n & 7) * 32 + 16);
    Mw = fmaxf(fmaxf(fmaxf(a[0], a[1]), fmaxf(a[2], a[3])), fmaxf(fmaxf(b[0], b[1]), fmaxf(b[2], b[3])));
  }
  {
    float ls = 0.f;
#pragma unroll
    for (int i = 0; i < 4; ++i) {
      float pv[8];
#pragma unroll
      for (int k = 0; k < 8; ++k) { pv[k] = __builtin_amdgcn_exp2f(sc[8 * i + k] - Mw); ls += pv[k]; }
      if (n < 8) *(LAS v4u*)(L + L_P + (w + 8 * i) * 512 + g * 128 + n * 16) = v4u{cvtpk(pv[0], pv[1]), cvtpk(pv[2], pv[3]), cvtpk(pv[4], pv[5]), cvtpk(pv[6], pv[7])};
    }
    ls = xrow_sum(ls);
    if (lane < 8) *(LAS float*)(L + L_LS + lane * 32 + w * 4) = ls;
  }
  lds_arrive(bar + 4, lane);
  lds_wait_ge(vbar + 4, 8);
  if (w == 0) {
    const f32x4 a = *(LAS f32x4*)(L + L_LS + (n & 7) * 32), b = *(LAS f32x4*)(L + L_LS + (n & 7) * 32 + 16);
    const float Lw = ((a[0] + a[1]) + (a[2] + a[3])) + ((b[0] + b[1]) + (b[2] + b[3]));
    if (lane < 8) __builtin_amdgcn_raw_buffer_store_b64(v2u{nosent(__float_as_uint(Mw)), nosent(__float_as_uint(Lw))}, wsr, WS_ST + (x * 32 + r) * 64 + lane * 8, 0, AUX_SC1);
  }
  ST(9);

  f32x4 oacc = {0.f, 0.f, 0.f, 0.f};
  const int pofs = L_P + g * 128 + (n & 7) * 16;
  auto pv_tile = [&](int t) {
    lds_wait_ge(fullw, G_V + 2 * t + 2);
    const v4u a = trfrag(L + ((2 * t) & 31) * 4096 + vtr, 4096);
    const v4u b = *(LAS v4u*)(L + pofs + t * 512);
    asm volatile("s_waitcnt lgkmcnt(0)" ::: "memory");
    if (lane == 0) ctrl[C_PROG + w] = 16u + 64 + 2 * t + 2;
    oacc = mfma16(a, b, oacc);
  };
#pragma unroll 1
  for (int t = 0; t < 20; ++t) {
    pv_tile(t);
    const int e = 20 * j + t, sg = e >= 2560, ii = e - 2560 * sg;
    const float hval = bflo((ii ? 0x3800u + ii : 0u) | (sg ? 0x8000u : 0u));
    float acc = 0.f;
#pragma unroll
    for (int i = 0; i < 7; ++i) acc = gelu_term(hval, mw1[i], mb1[i], mw2[i], acc);
    acc = wave_sum(acc);
    if (lane == 0) *(LAS float*)(L + L_TBP + t * 32 + w * 4) = acc;
  }
  ST(10);
  {
    asm volatile("s_waitcnt lgkmcnt(0)" ::: "memory");
    unsigned old = 0;
    if (lane == 0) old = __hip_atomic_fetch_add(bar + 5, 1u, __ATOMIC_RELAXED, __HIP_MEMORY_SCOPE_WORKGROUP);
    MEMBAR();
    if (__builtin_amdgcn_readfirstlane(old) == 7) {
      const int e = lane < 20 ? lane : 0;
      const f32x4 a = *(LAS f32x4*)(L + L_TBP + e * 32), b = *(LAS f32x4*)(L + L_TBP + e * 32 + 16);
      const float y = rbf(((a[0] + a[1]) + (a[2] + a[3])) + ((b[0] + b[1]) + (b[2] + b[3]))) + bflo(b2[0]);
      const float yo = dpp<XOR1>(y);
      if (lane < 20 && !(lane & 1)) __builtin_amdgcn_raw_buffer_store_b32(nosent(cvtpk(y, yo)), wsr, WS_TB + (20 * j + lane) * 2, 0, AUX_SC1);
    }
  }
  if (w == 1) {
    const int om = WS_MP + ((j >> 3) * 8 + (lane >> 3)) * 1024 + hb * 512 + ((j & 7) * 16 + 4 * tp) * 4;
    v4u pm;
    _Pragma("nounroll") for (unsigned i = 0; i < GCAP; ++i) {
      MEMBAR();
      pm = __builtin_amdgcn_raw_buffer_load_b128(wsr, om, 0, AUX_SC1);
      if (__all(umax4(pm) != SENT)) break;
      __builtin_amdgcn_s_sleep(4);
    }
    float mv[4];
#pragma unroll
    for (int i = 0; i < 4; ++i) { float s = __uint_as_float(pm[i]); s += dpp<ROR8>(s); mv[i] = xrow_sum(s); }
    if (lane < 8) __builtin_amdgcn_raw_buffer_store_b64(v2u{nosent(cvtpk(mv[0], mv[1])), nosent(cvtpk(mv[2], mv[3]))}, wsr, WS_MR + hb * 8192 + (16 * j + 4 * tp) * 2, 0, AUX_SC1);
  }
  float fh;
  {
    const int os = WS_ST + (x * 32 + (lane >> 3) * 4) * 64 + (lane & 7) * 8;
    v2u sv[4];
    _Pragma("nounroll") for (unsigned i = 0; i < GCAP; ++i) {
      MEMBAR();
#pragma unroll
      for (int k = 0; k < 4; ++k) sv[k] = __builtin_amdgcn_raw_buffer_load_b64(wsr, os + k * 64, 0, AUX_SC1);
      if (__all(max(max(max(sv[0][0], sv[0][1]), max(sv[1][0], sv[1][1])), max(max(sv[2][0], sv[2][1]), max(sv[3][0], sv[3][1]))) != SENT)) break;
      __builtin_amdgcn_s_sleep(4);
    }
    float M = fmaxf(fmaxf(__uint_as_float(sv[0][0]), __uint_as_float(sv[1][0])), fmaxf(__uint_as_float(sv[2][0]), __uint_as_float(sv[3][0])));
    M = fmaxf(M, dpp<ROR8>(M));
    M = xrow_max(M);
    float Ls = 0.f;
#pragma unroll
    for (int k = 0; k < 4; ++k) Ls = fmaf(__builtin_amdgcn_exp2f(__uint_as_float(sv[k][0]) - M), __uint_as_float(sv[k][1]), Ls);
    Ls += dpp<ROR8>(Ls);
    Ls = xrow_sum(Ls);
    fh = __builtin_amdgcn_exp2f(Mw - M) / Ls;
  }
  ST(11);
#pragma unroll 1
  for (int t = 20; t < 26; ++t) pv_tile(t);
  const int col = 128 * w + 8 * n + 2 * g;
  v4u tb0; unsigned tb1, fx, fga, fsb, fcb, fgb;
  if (j < 2) {
    fx = *(const unsigned*)(xin + j * D + col);
    _Pragma("nounroll") for (unsigned i = 0; i < GCAP; ++i) {
      MEMBAR();
      tb0 = __builtin_amdgcn_raw_buffer_load_b128(wsr, WS_TB + tid * 16, 0, AUX_SC1);
      tb1 = __builtin_amdgcn_raw_buffer_load_b32(wsr, WS_TB + 8192 + tid * 4, 0, AUX_SC1);
      fga = __builtin_amdgcn_raw_buffer_load_b32(wsr, WS_MR + j * 8192 + col * 2, 0, AUX_SC1);
      fsb = __builtin_amdgcn_raw_buffer_load_b32(wsr, WS_MR + j * 8192 + 2048 + col * 2, 0, AUX_SC1);
      fcb = __builtin_amdgcn_raw_buffer_load_b32(wsr, WS_MR + j * 8192 + 4096 + col * 2, 0, AUX_SC1);
      fgb = __builtin_amdgcn_raw_buffer_load_b32(wsr, WS_MR + j * 8192 + 6144 + col * 2, 0, AUX_SC1);
      if (__all(max(max(umax4(tb0), tb1), max(max(fga, fsb), max(fcb, fgb))) != SENT)) break;
      __builtin_amdgcn_s_sleep(4);
    }
  }
  ST(12);
#pragma unroll 1
  for (int t = 26; t < 32; ++t) pv_tile(t);
  ST(13);
  if (n < 8)
    __builtin_amdgcn_raw_buffer_store_b64(v2u{nosent(cvtpk(oacc[0] * fh, oacc[1] * fh)), nosent(cvtpk(oacc[2] * fh, oacc[3] * fh))}, wsr,
                                          WS_OR + (x * 32 + r) * 2048 + w * 256 + g * 64 + n * 8, 0, AUX_SC1);
  v4u wf[4];
  lds_wait_ge(fullw, G_WO + w + 1);
  {
    LAS unsigned char* a = L + w * 4096 + (8 * g + tq) * 128 + (tp & 1) * 8;
    const int ph = 2 * ((tq >> 1) | ((g & 1) << 1));
#pragma unroll
    for (int cb = 0; cb < 4; ++cb) wf[cb] = trfrag(a + (((2 * cb + (tp >> 1)) ^ ph) << 4), 512);
  }
  ST(14);
  {
    const int oa = WS_OR + (kvh_t * 32 + n) * 2048 + u_t * 512 + w * 64 + g * 16;
    v4u of[4];
    _Pragma("nounroll") for (unsigned i = 0; i < GCAP; ++i) {
      MEMBAR();
#pragma unroll
      for (int k = 0; k < 4; ++k) of[k] = __builtin_amdgcn_raw_buffer_load_b128(wsr, oa + (k >> 1) * (128 * 2048) + (k & 1) * (16 * 2048), 0, AUX_SC1);
      if (__all(max(max(umax4(of[0]), umax4(of[1])), max(umax4(of[2]), umax4(of[3]))) != SENT)) break;
      __builtin_amdgcn_s_sleep(2);
    }
    ST(15);
    float y[4];
#pragma unroll
    for (int cb = 0; cb < 4; ++cb) {
      const f32x4 z = {0.f, 0.f, 0.f, 0.f};
      const f32x4 a0 = mfma16(of[1], wf[cb], mfma16(of[0], wf[cb], z)), a1 = mfma16(of[3], wf[cb], mfma16(of[2], wf[cb], z));
      y[cb] = swap32_add((a0[0] + a0[1]) + (a0[2] + a0[3]), (a1[0] + a1[1]) + (a1[2] + a1[3]));
    }
    const float z0 = swap16_add(y[0], y[1]), z1 = swap16_add(y[2], y[3]);
    LAS float* t1 = (LAS float*)(L + L_T1R) + w * 128 + (g >> 1) * 64 + (g & 1) * 16 + n;
    t1[0] = z0; t1[32] = z1;
  }
  asm volatile("s_waitcnt lgkmcnt(0)" ::: "memory");
  __builtin_amdgcn_s_barrier();
  MEMBAR();
  if (w == 0) {
    f32x2 s = {0.f, 0.f};
#pragma unroll
    for (int k = 0; k < 8; ++k) s += *(LAS f32x2*)(L + L_T1R + k * 512 + lane * 8);
    __builtin_amdgcn_raw_buffer_store_b32(nosent(cvtpk(s[0], s[1])), wsr,
                                          WS_AP + ((lane >> 5) * 16 + kvh_t * 4 + u_t) * 2048 + (64 * cg_t + 2 * (lane & 31)) * 2, 0, AUX_SC1);
  }
  ST(16);
  if (j >= 2) return;

  *(LAS v4u*)(L + L_P + tid * 16) = tb0;
  *(LAS unsigned*)(L + L_P + 8192 + tid * 4) = tb1;
  v4u sel;
#pragma unroll
  for (int d = 0; d < 4; ++d) sel[d] = (d == (n >> 2) && !(n & 2)) ? (0x3F80u << (16 * (n & 1))) : 0u;
  f32x4 at;
  {
    const int ob = WS_AP + (j * 16 + 4 * g) * 2048 + (128 * w + 8 * n) * 2;
    v4u ap[4];
    _Pragma("nounroll") for (unsigned i = 0; i < GCAP; ++i) {
      MEMBAR();
#pragma unroll
      for (int k = 0; k < 4; ++k) ap[k] = __builtin_amdgcn_raw_buffer_load_b128(wsr, ob + k * 2048, 0, AUX_SC1);
      if (__all(max(max(umax4(ap[0]), umax4(ap[1])), max(umax4(ap[2]), umax4(ap[3]))) != SENT)) break;
      __builtin_amdgcn_s_sleep(1);
    }
    const f32x4 z = {0.f, 0.f, 0.f, 0.f};
    at = mfma16(sel, ap[1], mfma16(sel, ap[0], z)) + mfma16(sel, ap[3], mfma16(sel, ap[2], z));
  }
  ST(17);
  auto layernorm2 = [&](float& a, float& b, int slot) {
    const float mw = wave_sum(a + b) * (1.f / 128.f);
    const float m2 = wave_sum(fmaf(a - mw, a - mw, (b - mw) * (b - mw)));
    if (lane == 0) *(LAS f32x2*)(L + L_LNR + slot * 64 + w * 8) = f32x2{mw, m2};
    asm volatile("s_waitcnt lgkmcnt(0)" ::: "memory");
    __builtin_amdgcn_s_barrier();
    MEMBAR();
    f32x4 e[4];
#pragma unroll
    for (int k = 0; k < 4; ++k) e[k] = *(LAS f32x4*)(L + L_LNR + slot * 64 + k * 16);
    const float m = (((e[0][0] + e[0][2]) + (e[1][0] + e[1][2])) + ((e[2][0] + e[2][2]) + (e[3][0] + e[3][2]))) * 0.125f;
    float s2 = 0.f, dq = 0.f;
#pragma unroll
    for (int k = 0; k < 4; ++k) {
      s2 += e[k][1] + e[k][3];
      dq = fmaf(e[k][0] - m, e[k][0] - m, fmaf(e[k][2] - m, e[k][2] - m, dq));
    }
    const float var = fmaf(dq, 128.f, s2) * (1.f / 1024.f);
    const float mb = rbf(m), sd = rbf(sqrtf(rbf(rbf(var) + 1e-5f)));
    a = rbf(rbf(a - mb) / sd); b = rbf(rbf(b - mb) / sd);
  };
  const float x10 = rbf(bflo(fx) + rbf(bflo(fga) * rbf(at[0]))), x11 = rbf(bfhi(fx) + rbf(bfhi(fga) * rbf(at[1])));
  float h0 = x10, h1 = x11;
  layernorm2(h0, h1, 0);
  h0 = rbf(rbf(h0 * rbf(1.f + bflo(fcb))) + bflo(fsb));
  h1 = rbf(rbf(h1 * rbf(1.f + bfhi(fcb))) + bfhi(fsb));
  layernorm2(h0, h1, 1);
  float yv[2];
#pragma unroll
  for (int k = 0; k < 2; ++k) {
    const float hk = k ? h1 : h0;
    const unsigned bits = __float_as_uint(hk) >> 16, mag = bits & 0x7fffu, ii = mag - 0x3800u;
    const bool in = (mag == 0u) || (ii - 1u < 2559u);
    const unsigned idx = (mag == 0u || !in ? 0u : ii) + (bits >> 15) * 2560u;
    yv[k] = bflo(*(LAS u16*)(L + L_P + idx * 2));
    u64 todo = __ballot(!in);
    while (todo) {
      const int src = __builtin_ctzll(todo);
      todo &= todo - 1;
      const float hs = __builtin_bit_cast(float, __builtin_amdgcn_readlane(__builtin_bit_cast(int, hk), src));
      float acc = 0.f;
#pragma unroll 1
      for (int f = lane; f < F; f += 64) acc = gelu_term(hs, bflo(w1[f]), bflo(b1[f]), bflo(w2[f]), acc);
      const float y = rbf(rbf(wave_sum(acc)) + bflo(b2[0]));
      if (lane == src) yv[k] = y;
    }
  }
  *(unsigned*)(out + j * D + col) = cvtpk(x10 + rbf(bflo(fgb) * yv[0]), x11 + rbf(bfhi(fgb) * yv[1]));
  ST(18);
}

}

extern "C" void kernel_launch(void* const* d_in, const int* in_sizes, int n_in, void* d_out, int out_size, void* d_ws, size_t ws_size,
                              hipStream_t stream) {
  (void)hipFuncSetAttribute((const void*)fused_block, hipFuncAttributeMaxDynamicSharedMemorySize, L_BYTES);
  fused_block<<<256, 576, L_BYTES, stream>>>((const u16*)d_in[0], (const u16*)d_in[1], (const u16*)d_in[2], (const u16*)d_in[3],
                                             (const u16*)d_in[4], (const u16*)d_in[6], (const u16*)d_in[7], (const u16*)d_in[8],
                                             (const u16*)d_in[9], (const u16*)d_in[10], (const u16*)d_in[11], (u16*)d_out,
                                             (unsigned char*)d_ws);
}
```

```cpp
#define DELTAV 226
#include <hip/hip_runtime.h>
#include <stdint.h>

#define STAMP 0
#define CODEPF 8
#define PFPOL ""
#define DMIN 200
#define B2 0
#define PH 16
#define FINPF 1
#define WOGAP 0
#define DELTAW 240
#define TLBPF 0
#define SKEW 0
#define ALPHA 1
#define KA 8
#define DELTA0 DELTA16
#define RSL 24
#define TQ 580
#define DMINL 170
#define KW 9
#define KSLOW 24
#define WIN 56
#define W0 48
#define WSH 20
#define DELTA16 250
#define W0E 48
#define ANCHOR 10
#define DLY1 18
#define DLY2 18
#define DLYA 24
#define DLYB 0

namespace {

typedef unsigned short u16;
typedef unsigned long long u64;
typedef unsigned v4u __attribute__((ext_vector_type(4)));
typedef unsigned v2u __attribute__((ext_vector_type(2)));
typedef float f32x4 __attribute__((ext_vector_type(4)));
typedef float f32x2 __attribute__((ext_vector_type(2)));
typedef short bf16x8 __attribute__((ext_vector_type(8)));
typedef short s16x4 __attribute__((ext_vector_type(4)));
typedef __bf16 bf16x2_t __attribute__((ext_vector_type(2)));
#define LAS __attribute__((address_space(3)))
#define GAS __attribute__((address_space(1)))

constexpr int D = 1024, SK = 32768, F = 3584;
constexpr unsigned SENT = 0xFFFFFFFFu;
constexpr int AUX_SC1 = 16;
constexpr unsigned GCAP = 1u << 17, LCAP = 1u << 22;

constexpr int WS_X = 0;
constexpr int WS_RDY = 4096;
constexpr int WS_RDG = 12288;
constexpr int WS_P1 = 16384;
constexpr int WS_QP = WS_P1 + 131072;
constexpr int WS_MP = WS_QP + 262144;
constexpr int WS_CLR = WS_MP + 262144;
constexpr int WS_OR = WS_CLR;
constexpr int WS_AP = WS_OR + 524288;
constexpr int WS_MR = WS_AP + 65536;
constexpr int WS_ST = WS_MR + 16384;
constexpr int WS_TB = WS_ST + 16384;
constexpr int WS_END = WS_TB + 16384;
constexpr int CLR_PER_WG = (WS_END - WS_CLR) / 256;
static_assert(CLR_PER_WG == 2496, "clear share");
constexpr int WS_STAMP = 2 * 1024 * 1024;

constexpr int L_WQ = 16 * 4096, L_MRS = 24 * 4096;
constexpr int L_P = 131072;
constexpr int L_TB = L_P;
constexpr int L_CTRL = L_P + 16384;
constexpr int C_FULL = 0, C_RDY = 1, C_STAT = 2, C_FH = 3, C_TB = 4, C_TAG = 5, C_CLR = 6, C_T0 = 7, C_PROG = 8, C_TE = 12, C_BAR = 16, C_DUMMY = 64;
constexpr int L_SV = L_CTRL + 512, L_HV = L_SV + 512, L_QV = L_HV + 512;
constexpr int L_T1R = L_SV, L_OV = L_SV + 2048;
constexpr int L_MX = L_QV + 2176, L_LS = L_MX + 128, L_LNS = L_LS + 128, L_FH = L_LNS + 16, L_TBP = L_FH + 32;
constexpr int L_LNR = L_TBP + 352, L_BYTES = L_LNR + 64 + 256;
static_assert(L_BYTES + 1152 <= 163840 && (L_QV % 16) == 0 && (L_MX % 16) == 0 && (L_TBP % 16) == 0 && (L_LNR % 16) == 0, "LDS map");

static_assert(KSLOW > 8 && KSLOW < 32 && WIN >= 16 && WIN <= 60 && WIN % 4 == 0, "window");
constexpr int G_S1 = 0, G_WQ = 4, G_MR = 12, G_K = 20, G_V = 84, G_WO = 148, G_END = 156;

__device__ __forceinline__ float bflo(unsigned x) { return __uint_as_float(x << 16); }
__device__ __forceinline__ float bfhi(unsigned x) { return __uint_as_float(x & 0xffff0000u); }
__device__ __forceinline__ f32x2 unpk(unsigned x) { return f32x2{bflo(x), bfhi(x)}; }
__device__ __forceinline__ unsigned cvtpk(float lo, float hi) {
  bf16x2_t v = {(__bf16)lo, (__bf16)hi};
  return __builtin_bit_cast(unsigned, v);
}
__device__ __forceinline__ unsigned cvtpk(f32x2 v) { return cvtpk(v[0], v[1]); }
__device__ __forceinline__ float rbf(float x) { return bflo(cvtpk(x, x)); }
__device__ __forceinline__ f32x2 rbf(f32x2 v) { unsigned p = cvtpk(v); asm("" : "+v"(p)); return unpk(p); }
__device__ __forceinline__ unsigned nosent(unsigned u) { return u < 0xFFFFFFFEu ? u : 0xFFFFFFFEu; }
__device__ __forceinline__ v4u nosent4(f32x4 v) {
  return v4u{nosent(__float_as_uint(v[0])), nosent(__float_as_uint(v[1])), nosent(__float_as_uint(v[2])), nosent(__float_as_uint(v[3]))};
}
__device__ __forceinline__ unsigned umax4(v4u v, unsigned m) { return max(max(max(max(m, v[0]), v[1]), v[2]), v[3]); }
__device__ __forceinline__ f32x4 asf(v4u v) { return __builtin_bit_cast(f32x4, v); }
__device__ __forceinline__ f32x4 mfma16(v4u a, v4u b, f32x4 c) {
  return __builtin_amdgcn_mfma_f32_16x16x32_bf16(__builtin_bit_cast(bf16x8, a), __builtin_bit_cast(bf16x8, b), c, 0, 0, 0);
}
template <int CTRL> __device__ __forceinline__ float dpp(float x) {
  return __builtin_bit_cast(float, __builtin_amdgcn_mov_dpp(__builtin_bit_cast(int, x), CTRL, 0xf, 0xf, true));
}
constexpr int XOR1 = 0xB1, XOR2 = 0x4E, XOR7 = 0x141, ROR8 = 0x128;
__device__ __forceinline__ float swap32_add(float a, float b) {
  auto s = __builtin_amdgcn_permlane32_swap(__float_as_uint(a), __float_as_uint(b), false, false);
  return __uint_as_float(s[0]) + __uint_as_float(s[1]);
}
__device__ __forceinline__ float swap16_add(float a, float b) {
  auto s = __builtin_amdgcn_permlane16_swap(__float_as_uint(a), __float_as_uint(b), false, false);
  return __uint_as_float(s[0]) + __uint_as_float(s[1]);
}
__device__ __forceinline__ float xrow_sum(float x) { x = swap16_add(x, x); return swap32_add(x, x); }
__device__ __forceinline__ float xrow_max(float x) {
  auto s = __builtin_amdgcn_permlane16_swap(__float_as_uint(x), __float_as_uint(x), false, false);
  x = fmaxf(__uint_as_float(s[0]), __uint_as_float(s[1]));
  auto t = __builtin_amdgcn_permlane32_swap(__float_as_uint(x), __float_as_uint(x), false, false);
  return fmaxf(__uint_as_float(t[0]), __uint_as_float(t[1]));
}
__device__ __forceinline__ float row_sum(float x) { x += dpp<XOR1>(x); x += dpp<XOR2>(x); x += dpp<XOR7>(x); return x + dpp<ROR8>(x); }
__device__ __forceinline__ float wave_sum(float x) { return xrow_sum(row_sum(x)); }
__device__ __forceinline__ void sum2_to_last(float& a, float& b) {
#define S2(c) "v_add_f32_dpp %0, %0, %0 " c " bank_mask:0xf\n\tv_add_f32_dpp %1, %1, %1 " c " bank_mask:0xf\n\ts_nop 0\n\t"
  asm("s_nop 1\n\t" S2("quad_perm:[1,0,3,2] row_mask:0xf") S2("quad_perm:[2,3,0,1] row_mask:0xf") S2("row_half_mirror row_mask:0xf")
      S2("row_ror:8 row_mask:0xf") S2("row_bcast:15 row_mask:0xa")
      "v_add_f32_dpp %0, %0, %0 row_bcast:31 row_mask:0xc bank_mask:0xf\n\tv_add_f32_dpp %1, %1, %1 row_bcast:31 row_mask:0xc bank_mask:0xf" : "+v"(a), "+v"(b));
#undef S2
}
__device__ __forceinline__ v2u pk4(f32x4 v) { v2u p = {cvtpk(v[0], v[1]), cvtpk(v[2], v[3])}; asm("" : "+v"(p)); return p; }
__device__ __forceinline__ f32x4 unpk4(v2u p) { return f32x4{bflo(p[0]), bfhi(p[0]), bflo(p[1]), bfhi(p[1])}; }
__device__ __forceinline__ f32x4 rbf(f32x4 v) { return unpk4(pk4(v)); }
__device__ __forceinline__ u64 rt() { return __builtin_amdgcn_s_memrealtime(); }

__device__ __forceinline__ v2u trread(LAS unsigned char* p) {
  return __builtin_bit_cast(v2u, __builtin_amdgcn_ds_read_tr16_b64_v4i16((LAS s16x4*)p));
}
__device__ __forceinline__ v4u trfrag(LAS unsigned char* p, int step) {
  const v2u a = trread(p), b = trread(p + step);
  return v4u{a[0], a[1], b[0], b[1]};
}
#define MEMBAR() asm volatile("" ::: "memory")
#define LGKM0() asm volatile("s_waitcnt lgkmcnt(0)" ::: "memory")

template <int SLEEP>
__device__ __forceinline__ unsigned lds_wait_ge(volatile LAS unsigned* w, unsigned need) {
  unsigned v;
  _Pragma("nounroll") for (unsigned i = 0; i < LCAP; ++i) {
    v = __builtin_amdgcn_readfirstlane(*w);
    if (v >= need) break;
    if (SLEEP) __builtin_amdgcn_s_sleep(SLEEP);
  }
  MEMBAR();
  return v;
}
__device__ __forceinline__ void lds_arrive(LAS unsigned char* L, int idx) {
  asm volatile("s_waitcnt lgkmcnt(0)\n\ts_mov_b64 exec, 1\n\tds_add_u32 %0, %1\n\ts_mov_b64 exec, -1"
               :: "v"((unsigned)(size_t)(L + L_CTRL + 4 * (C_BAR + idx))), "v"(1u) : "memory");
}

#define LD_PUBLISH "s_getreg_b32 %[c], hwreg(HW_REG_IB_STS, 22, 2)\n\ts_getreg_b32 %[t2], hwreg(HW_REG_IB_STS, 0, 4)\n\ts_lshl4_add_u32 %[c], %[c], %[t2]\n\t" \
                   "s_sub_i32 %[t1], %[P], %[c]\n\ts_max_i32 %[t1], %[t1], 0\n\tv_mov_b32 v63, %[t1]\n\tds_write_b32 %[fw], v63\n\t"
template <int NP, bool RING>
__device__ __forceinline__ unsigned stream_groups(const char* sb, unsigned step, unsigned ld, int cnt, int& P, unsigned& due, unsigned& now, unsigned& nxt, unsigned& c,
                                              int& freed, unsigned v0, unsigned v1, unsigned v2, unsigned v3, unsigned fwaddr, unsigned pgaddr, int lag = 0,
                                              unsigned dlt = DELTA0, unsigned w0 = W0, unsigned dmin = DMIN, int psw = 0) {
  unsigned t0, t1, t2, cap = LCAP;
  w0 -= NP;
  asm volatile(
      "s_mov_b64 s[90:91], %[sb]\n\ts_branch 1f\n"
      "2:\n\t"
      "ds_read_b32 v63, %[pg]\n\ts_waitcnt lgkmcnt(0)\n\t"
      "v_min_u32_dpp v63, v63, v63 quad_perm:[1,0,3,2] row_mask:0xf bank_mask:0xf\n\ts_nop 1\n\t"
      "v_min_u32_dpp v63, v63, v63 quad_perm:[2,3,0,1] row_mask:0xf bank_mask:0xf\n\ts_nop 0\n\t"
      "v_readfirstlane_b32 %[freed], v63\n\t"
      "s_sub_u32 %[cap], %[cap], 1\n\ts_cbranch_scc1 5f\n"
      "4:\n\t"
      "s_memrealtime s[92:93]\n\ts_waitcnt lgkmcnt(0)\n\t" LD_PUBLISH
      "s_lshl_b32 %[now], s92, 4\n"
      "1:\n\t"
      ".if %[ring]\n\ts_sub_i32 %[t0], %[P], %[lag]\n\ts_cmp_lt_i32 %[freed], %[t0]\n\ts_cbranch_scc1 2b\n\t.endif\n\t"
      "s_sub_u32 %[t1], %[now], %[nxt]\n\ts_cmp_lt_i32 %[t1], 0\n\ts_cbranch_scc1 4b\n\t"
      "s_sub_u32 %[t1], %[now], %[due]\n\ts_cmp_lt_i32 %[t1], 0\n\ts_cbranch_scc1 4b\n\t"
      "s_lshr_b32 %[t1], %[t1], %[sh]\n\ts_add_u32 %[t1], %[t1], %[w0]\n\ts_cmp_gt_u32 %[c], %[t1]\n\ts_cbranch_scc1 4b\n"
      "5:\n\t"
      "s_mov_b32 m0, %[ld]\n\ts_waitcnt vmcnt(%[w])\n\ts_memrealtime s[92:93]\n\t"
      ".if %[np] == 8\n\t"
      "global_load_lds_dwordx4 %[v0], s[90:91] offset:-4096 nt\n\tglobal_load_lds_dwordx4 %[v1], s[90:91] offset:-3072 nt\n\t"
      "global_load_lds_dwordx4 %[v2], s[90:91] offset:-2048 nt\n\tglobal_load_lds_dwordx4 %[v3], s[90:91] offset:-1024 nt\n\t"
      ".endif\n\t"
      "global_load_lds_dwordx4 %[v0], s[90:91] nt\n\tglobal_load_lds_dwordx4 %[v1], s[90:91] offset:1024 nt\n\t"
      "global_load_lds_dwordx4 %[v2], s[90:91] offset:2048 nt\n\tglobal_load_lds_dwordx4 %[v3], s[90:91] offset:3072 nt\n\t"
      "s_add_u32 %[P], %[P], %[np]\n\ts_cmp_eq_u32 %[P], %[psw]\n\ts_cselect_b32 %[w0], %[we], %[w0]\n\ts_add_u32 s90, s90, %[step]\n\ts_addc_u32 s91, s91, 0\n\t"
      "s_add_u32 %[due], %[due], %[dlt]\n\ts_add_u32 %[ld], %[ld], %[np] * 1024\n\t"
      ".if %[ring]\n\ts_and_b32 %[ld], %[ld], 0x1ffff\n\t.endif\n\t"
      "s_waitcnt lgkmcnt(0)\n\t" LD_PUBLISH
      "s_lshl_b32 %[now], s92, 4\n\ts_add_u32 %[nxt], %[nxt], %[dmin]\n\ts_sub_u32 %[t1], %[now], %[nxt]\n\ts_cmp_gt_i32 %[t1], 0\n\t"
      "s_cselect_b32 %[nxt], %[now], %[nxt]\n\ts_sub_u32 %[cnt], %[cnt], 1\n\ts_cmp_lg_u32 %[cnt], 0\n\ts_cbranch_scc1 1b"
      : [ld] "+&s"(ld), [cnt] "+&s"(cnt), [P] "+&s"(P), [due] "+&s"(due), [now] "+&s"(now), [freed] "+&s"(freed), [cap] "+&s"(cap), [c] "+&s"(c), [nxt] "+&s"(nxt), [w0] "+&s"(w0),
        [t0] "=&s"(t0), [t1] "=&s"(t1), [t2] "=&s"(t2)
      : [sb] "s"(sb), [step] "s"(step), [v0] "v"(v0), [v1] "v"(v1), [v2] "v"(v2), [v3] "v"(v3), [fw] "v"(fwaddr), [pg] "v"(pgaddr),
        [w] "i"(WIN - NP), [psw] "s"(psw), [we] "i"(W0E - NP), [sh] "i"(WSH), [dlt] "s"(dlt * (NP / 4)), [dmin] "s"(dmin * (NP / 4)), [lag] "s"(lag), [ring] "i"(RING ? 1 : 0), [np] "i"(NP)
      : "s90", "s91", "s92", "s93", "v63", "scc", "memory");
  return LCAP - cap;
}
__device__ __forceinline__ void stream_drain(int P, unsigned fwaddr) {
  unsigned t1, t2, c, cap = LCAP;
  asm volatile("1:\n\t" LD_PUBLISH
               "s_cmp_ge_i32 %[t1], %[P]\n\ts_cbranch_scc1 2f\n\ts_waitcnt lgkmcnt(0)\n\ts_sub_u32 %[cap], %[cap], 1\n\ts_cbranch_scc0 1b\n2:"
               : [cap] "+&s"(cap), [t1] "=&s"(t1), [t2] "=&s"(t2), [c] "=&s"(c) : [P] "s"(P), [fw] "v"(fwaddr) : "v63", "scc", "memory");
}

constexpr unsigned TB0 = 0x3680u, TBN = 2816u;
constexpr float GELU_A = -2.302208198f, GELU_B = -0.1029432397f;
__device__ __forceinline__ float gelu_term(float h, float w1, float b1, float w2, float acc) {
  const float u = fmaf(h, w1, b1);
  const float z = u * fmaf(u * u, GELU_B, GELU_A);
  return fmaf(u * __builtin_amdgcn_rcpf(1.f + __builtin_amdgcn_exp2f(z)), w2, acc);
}

#define ST(i) do {} while (0)
#define STN(i, v) do {} while (0)
#define STDUMP() do {} while (0)

__global__ void __launch_bounds__(576, 3)
fused_block(const u16* __restrict__ xin, const u16* __restrict__ kc, const u16* __restrict__ vc, const u16* __restrict__ wq,
            const u16* __restrict__ wo, const u16* __restrict__ w1, const u16* __restrict__ b1, const u16* __restrict__ w2,
            const u16* __restrict__ b2, const u16* __restrict__ cin, const u16* __restrict__ wmod, u16* __restrict__ out,
            unsigned char* ws) {
  extern __shared__ __attribute__((aligned(16))) unsigned char lds_raw[];
  LAS unsigned char* L = (LAS unsigned char*)lds_raw;
  volatile LAS unsigned* ctrl = (volatile LAS unsigned*)(L + L_CTRL);
  const int j = blockIdx.x, x = j & 7, r = j >> 3;
  const int lane = threadIdx.x & 63, w = __builtin_amdgcn_readfirstlane(threadIdx.x >> 6);
  const int ks = r & 7, combo = r >> 3;
  const int kvh_t = x >> 1, u_t = (x & 1) * 2 + (r >> 4), cg_t = r & 15;
  {
    u64 pc0; int cl1, cl2;
    asm volatile("s_getpc_b64 %0\n.Lfb_begin:\n\ts_mov_b32 %1, .Lfb_end1-.Lfb_begin\n\ts_mov_b32 %2, .Lfb_end2-.Lfb_begin" : "=s"(pc0), "=s"(cl1), "=s"(cl2));
    if (w == 5 && j < CODEPF) {
      const int len = max(cl1, cl2);
      _Pragma("nounroll") for (int o = 0; o < len; o += 8192)
        asm volatile("s_mov_b32 m0, %2\n\ts_nop 0\n\tglobal_load_lds_dword %1, %0" PFPOL :: "s"(pc0), "v"(min(o + lane * 128, len - 4) & ~3), "s"((unsigned)(size_t)L + L_BYTES - 256) : "memory");
    }
  }
  if (w == 0) { ctrl[lane] = 0u; LGKM0(); }
  asm volatile("" :: "s"(xin), "s"(kc), "s"(vc), "s"(wq), "s"(wo), "s"(w1), "s"(b1), "s"(w2), "s"(b2), "s"(cin), "s"(wmod), "s"(out), "s"(ws));
  __builtin_amdgcn_s_barrier();
  MEMBAR();

  const int n = lane & 15, g = lane >> 4, tq = (lane >> 2) & 3, tp = lane & 3, hb = (lane >> 2) & 1;
  const unsigned l0 = (unsigned)(size_t)L;
  if (w < 8) {
  const auto wsr = __builtin_amdgcn_make_buffer_rsrc(ws, 0, WS_END, 0x00020000);
  volatile LAS unsigned* vbar = ctrl + C_BAR;

  if (w >= 4) {
    if (w == 4) {
      __builtin_amdgcn_s_setprio(2);
      lds_wait_ge<1>(ctrl + C_CLR, 1);
      lds_wait_ge<1>(ctrl + C_FULL, 12);
      const unsigned tag = ctrl[C_TAG];
      ST(22);
      _Pragma("nounroll") for (unsigned i = 0; i < GCAP; ++i) {
        MEMBAR();
        if (__all(__builtin_amdgcn_raw_buffer_load_b32(wsr, WS_RDG + (x * 32 + (lane & 31)) * 16, 0, AUX_SC1) == tag)) break;
        __builtin_amdgcn_s_sleep(4);
      }
      ctrl[lane == 0 ? C_RDY : C_DUMMY + lane] = 1u;
      ST(29);
      v2u an[4];
      _Pragma("nounroll") for (unsigned i = 0; i < GCAP; ++i) {
        MEMBAR();
        bool ok = true;
#pragma unroll
        for (int k = 0; k < 4; ++k) { an[k] = __builtin_amdgcn_raw_buffer_load_b64(wsr, WS_RDY + (k * 64 + lane) * 32, 0, AUX_SC1); ok &= an[k][0] == tag; }
        if (__all(ok)) break;
        __builtin_amdgcn_s_sleep(RSL);
      }
      {
        const unsigned t0 = __builtin_amdgcn_readfirstlane(an[0][1]);
        const float d0 = (float)(int)(an[0][1] - t0), d1 = (float)(int)(an[1][1] - t0), d2 = (float)(int)(an[2][1] - t0), d3 = (float)(int)(an[3][1] - t0);
        float dm = fmaxf(fmaxf(d0, d1), fmaxf(d2, d3));
        dm = fmaxf(dm, dpp<XOR1>(dm)); dm = fmaxf(dm, dpp<XOR2>(dm)); dm = fmaxf(dm, dpp<XOR7>(dm)); dm = xrow_max(fmaxf(dm, dpp<ROR8>(dm)));
        const float da = wave_sum((d0 + d1) + (d2 + d3)) * (1.f / 256.f);
        ctrl[lane == 0 ? C_TE : C_DUMMY + lane] = t0 + (int)dm;
        ctrl[lane == 0 ? C_T0 : C_DUMMY + lane] = (t0 + (int)(da + (dm - da) * (ALPHA / 8.f))) | 1u;
      }
      ctrl[lane == 0 ? C_RDY : C_DUMMY + lane] = 2u;
      if (j == 0 && lane == 0) __builtin_amdgcn_raw_buffer_store_b32(tag, wsr, WS_X, 0, AUX_SC1);
      __builtin_amdgcn_s_setprio(0);
      ST(24);
    }
    lds_wait_ge<4>(ctrl + C_FULL, 4);
    const int t4 = (w - 4) * 64 + lane;
    f32x2 mw1[7], mb1[7], mw2[7];
#pragma unroll
    for (int i = 0; i < 7; ++i) {
      mw1[i] = unpk(((const unsigned*)w1)[t4 + 256 * i]); mb1[i] = unpk(((const unsigned*)b1)[t4 + 256 * i]); mw2[i] = unpk(((const unsigned*)w2)[t4 + 256 * i]);
    }
    const float bias2 = bflo(b2[0]);
#pragma unroll 1
    for (int t = 0; t < 22; ++t) {
      const unsigned e = 22 * j + t, ii = e >> 1;
      const float hval = bflo((ii ? TB0 + ii : 0u) | ((e & 1) << 15));
      f32x2 acc = {0.f, 0.f};
#pragma unroll
      for (int i = 0; i < 7; ++i) {
        const f32x2 u = __builtin_elementwise_fma(f32x2{hval, hval}, mw1[i], mb1[i]);
        const f32x2 z = u * __builtin_elementwise_fma(u * u, f32x2{GELU_B, GELU_B}, f32x2{GELU_A, GELU_A});
        const f32x2 d = f32x2{__builtin_amdgcn_exp2f(z[0]), __builtin_amdgcn_exp2f(z[1])} + 1.f;
        acc = __builtin_elementwise_fma(u * mw2[i], f32x2{__builtin_amdgcn_rcpf(d[0]), __builtin_amdgcn_rcpf(d[1])}, acc);
      }
      const float s = wave_sum(acc[0] + acc[1]);
      if (lane == 0) *(LAS float*)(L + L_TBP + t * 16 + (w - 4) * 4) = s;
    }
    ST(25);
    lds_wait_ge<2>(ctrl + C_RDY, 2);
    {
      LGKM0();
      unsigned old = 0;
      if (lane == 0) old = __hip_atomic_fetch_add((LAS unsigned*)(L + L_CTRL) + C_BAR + 5, 1u, __ATOMIC_RELAXED, __HIP_MEMORY_SCOPE_WORKGROUP);
      MEMBAR();
      if (__builtin_amdgcn_readfirstlane(old) == 3) {
        const f32x4 a = *(LAS f32x4*)(L + L_TBP + (lane < 22 ? lane : 0) * 16);
        const float y = rbf((a[0] + a[1]) + (a[2] + a[3])) + bias2;
        const float yo = dpp<XOR1>(y);
        if (lane < 22 && !(lane & 1)) __builtin_amdgcn_raw_buffer_store_b32(nosent(cvtpk(y, yo)), wsr, WS_TB + (22 * j + lane) * 2, 0, AUX_SC1);
      }
    }
    if (w == 5) {
      const int om = WS_MP + ((j >> 3) * 8 + (lane >> 3)) * 1024 + hb * 512 + ((j & 7) * 16 + 4 * tp) * 4;
      v4u pm;
      _Pragma("nounroll") for (unsigned i = 0; i < GCAP; ++i) {
        MEMBAR();
        pm = __builtin_amdgcn_raw_buffer_load_b128(wsr, om, 0, AUX_SC1);
        if (__all(umax4(pm, 0) != SENT)) break;
        __builtin_amdgcn_s_sleep(16);
      }
      float mv[4];
#pragma unroll
      for (int i = 0; i < 4; ++i) { float s = __uint_as_float(pm[i]); s += dpp<ROR8>(s); mv[i] = xrow_sum(s); }
      if (lane < 8) __builtin_amdgcn_raw_buffer_store_b64(v2u{nosent(cvtpk(mv[0], mv[1])), nosent(cvtpk(mv[2], mv[3]))}, wsr, WS_MR + hb * 8192 + (16 * j + 4 * tp) * 2, 0, AUX_SC1);
      ST(26);
    } else if (w == 6) {
      lds_wait_ge<8>(ctrl + C_STAT, 1);
      const int os = WS_ST + (x * 32 + (lane >> 3) * 4) * 64 + (lane & 7) * 8;
      v2u sv[4];
      _Pragma("nounroll") for (unsigned i = 0; i < GCAP; ++i) {
        if (i) __builtin_amdgcn_s_sleep(12); else __builtin_amdgcn_s_sleep(48);
        MEMBAR();
#pragma unroll
        for (int k = 0; k < 4; ++k) sv[k] = __builtin_amdgcn_raw_buffer_load_b64(wsr, os + k * 64, 0, AUX_SC1);
        if (__all(umax4(v4u{sv[0][0], sv[0][1], sv[1][0], sv[1][1]}, umax4(v4u{sv[2][0], sv[2][1], sv[3][0], sv[3][1]}, 0)) != SENT)) break;
      }
      float M = fmaxf(fmaxf(__uint_as_float(sv[0][0]), __uint_as_float(sv[1][0])), fmaxf(__uint_as_float(sv[2][0]), __uint_as_float(sv[3][0])));
      M = fmaxf(M, dpp<ROR8>(M));
      M = xrow_max(M);
      float Ls = 0.f;
#pragma unroll
      for (int k = 0; k < 4; ++k) Ls = fmaf(__builtin_amdgcn_exp2f(__uint_as_float(sv[k][0]) - M), __uint_as_float(sv[k][1]), Ls);
      Ls += dpp<ROR8>(Ls);
      Ls = xrow_sum(Ls);
      volatile LAS float* fhp = (volatile LAS float*)(L + L_FH) + (lane & 7);
      const float fh = __builtin_amdgcn_exp2f(*fhp - M) / Ls;
      if (lane < 8) *fhp = fh;
      LGKM0();
      ctrl[lane == 0 ? C_FH : C_DUMMY + lane] = 1u;
      ST(27);
    } else if (w == 7 && j < 2) {
      lds_wait_ge<8>(ctrl + C_STAT, 1);
      v4u tv[11];
      _Pragma("nounroll") for (unsigned i = 0; i < GCAP; ++i) {
        MEMBAR();
        unsigned m = 0;
#pragma unroll
        for (int k = 0; k < 11; ++k) tv[k] = __builtin_amdgcn_raw_buffer_load_b128(wsr, WS_TB + lane * 16, k * 1024, AUX_SC1);
#pragma unroll
        for (int k = 0; k < 11; ++k) m = umax4(tv[k], m);
        if (__all(m != SENT)) break;
        __builtin_amdgcn_s_sleep(32);
      }
#pragma unroll
      for (int k = 0; k < 4; ++k) lds_wait_ge<2>(ctrl + C_PROG + k, 4 * (16 + 128));
#pragma unroll
      for (int k = 0; k < 11; ++k) *(LAS v4u*)(L + L_TB + k * 1024 + lane * 16) = tv[k];
      LGKM0();
      ctrl[lane == 0 ? C_TB : C_DUMMY + lane] = 1u;
      ST(28);
    }
    STDUMP();
    asm volatile(".Lfb_end1:");
    return;
  }

  __builtin_amdgcn_s_setprio(2);
  ST(11);
  volatile LAS unsigned* progw = ctrl + (lane == 0 ? C_PROG + w : C_DUMMY + lane);
  unsigned fullc = 0;
  auto wait_full = [&](unsigned need) { if (fullc < 4 * need) fullc = lds_wait_ge<0>(ctrl + C_FULL, 4 * need); };
  const unsigned xh = *(const unsigned*)(xin + hb * D + 128 * x + 32 * w + 16 * (g >> 1) + 4 * tp + 2 * (g & 1));
  const v4u* sp = (const v4u*)(w == 0 ? cin + (lane >> 5) * D + 128 * ks + 8 * n : xin + ((w - 1) & 1) * D + lane * 16);
  const v4u sa = sp[0], sb_ = sp[w == 0 ? 0 : 1];
  if (w == 0) {
    unsigned o[4];
#pragma unroll
    for (int i = 0; i < 4; ++i) {
      const f32x2 c = unpk(sa[i]), z = c * -1.4426950408889634f;
      o[i] = cvtpk(c * f32x2{__builtin_amdgcn_rcpf(1.f + __builtin_amdgcn_exp2f(z[0])), __builtin_amdgcn_rcpf(1.f + __builtin_amdgcn_exp2f(z[1]))});
    }
    LAS unsigned char* d = L + L_SV + (lane >> 5) * 256 + (n >> 2) * 64 + (n & 1) * 32 + ((n >> 1) & 1) * 8;
    *(LAS v2u*)d = v2u{o[0], o[1]}; *(LAS v2u*)(d + 16) = v2u{o[2], o[3]};
  } else if (w < 3) {
    f32x2 v[8];
#pragma unroll
    for (int i = 0; i < 4; ++i) { v[i] = unpk(sa[i]); v[4 + i] = unpk(sb_[i]); }
    const f32x2 s = ((v[0] + v[1]) + (v[2] + v[3])) + ((v[4] + v[5]) + (v[6] + v[7]));
    const float m = wave_sum(s[0] + s[1]) * (1.f / 1024.f);
    f32x2 q = {0.f, 0.f};
#pragma unroll
    for (int i = 0; i < 8; ++i) q = __builtin_elementwise_fma(v[i] - m, v[i] - m, q);
    const float var = wave_sum(q[0] + q[1]) * (1.f / 1024.f);
    if (lane == 0) *(LAS f32x2*)(L + L_LNS + (w - 1) * 8) = f32x2{rbf(m), __builtin_amdgcn_rcpf(rbf(__builtin_amdgcn_sqrtf(rbf(rbf(var) + 1e-5f))))};
  }
  lds_arrive(L, 0);
  ST(1);
  lds_wait_ge<0>(vbar + 0, 4);
  v4u sf[4];
#pragma unroll
  for (int st = 0; st < 4; ++st) sf[st] = *(LAS v4u*)(L + L_SV + (n & 1) * 256 + st * 64 + g * 16);
  {
    wait_full(G_S1 + 4);
    ST(23);
    LAS unsigned char* a = L + L_P + (4 * g + tq) * 128 + (((2 * w + (tp >> 1)) ^ (2 * (2 * (g & 1) + (tq >> 1)))) << 4) + (tp & 1) * 8;
    f32x4 acc = {0.f, 0.f, 0.f, 0.f};
#pragma unroll
    for (int st = 0; st < 4; ++st) acc = mfma16(trfrag(a + st * 4096, 2048), sf[st], acc);
    if (n < 2)
      __builtin_amdgcn_raw_buffer_store_b128(nosent4(acc), wsr, WS_P1 + ((x * 8 + ks) * 2 + (combo >> 1)) * 1024 + n * 512 + ((combo & 1) * 64 + 16 * w + 4 * g) * 4, 0, AUX_SC1);
  }
  ST(2);
  const int vtr0 = (4 * g + tq) * 256 + (((4 * w + (tp >> 1)) ^ (2 * (4 * (g & 1) + tq))) << 4) + (tp & 1) * 8, vtr1 = vtr0 ^ 32;
  v4u wqf[8];
  lds_wait_ge<0>(ctrl + C_RDY, 1);
  ST(3);
  {
    const int o1 = WS_P1 + (x * 8 + (lane >> 3)) * 2048 + hb * 512 + (32 * w + 4 * tp) * 4;
    v4u pp[4];
    __builtin_amdgcn_s_sleep(DLY1);
    _Pragma("nounroll") for (unsigned i = 0; i < GCAP; ++i) {
      MEMBAR();
#pragma unroll
      for (int k = 0; k < 4; ++k) pp[k] = __builtin_amdgcn_raw_buffer_load_b128(wsr, o1 + (k >> 1) * 1024 + (k & 1) * 64, 0, AUX_SC1);
      STN(19, i + 1);
      if (__all(umax4(pp[0], umax4(pp[1], umax4(pp[2], umax4(pp[3], 0)))) != SENT)) break;
      __builtin_amdgcn_s_sleep(4);
    }
    f32x2 mv[2];
#pragma unroll
    for (int k = 0; k < 2; ++k) {
      float y[4];
#pragma unroll
      for (int i = 0; i < 4; ++i) y[i] = swap32_add(__uint_as_float(pp[2 * k][i]), __uint_as_float(pp[2 * k + 1][i]));
      const float a = swap16_add(y[0], y[2]), b = swap16_add(y[1], y[3]);
      mv[k] = rbf(f32x2{a + dpp<ROR8>(a), b + dpp<ROR8>(b)});
    }
    const f32x2 ln = *(LAS f32x2*)(L + L_LNS + hb * 8);
    const f32x2 nv = rbf(rbf(unpk(xh) - ln[0]) * ln[1]);
    const unsigned hp = cvtpk(rbf(nv * rbf(1.f + mv[1])) + mv[0]);
    if (!(lane & 8)) *(LAS unsigned*)(L + L_HV + hb * 256 + w * 64 + tp * 16 + (g >> 1) * 8 + (g & 1) * 4) = hp;
  }
  ST(4);
  lds_arrive(L, 1);
  wait_full(G_WQ + 8);
#pragma unroll
  for (int st = 0; st < 4; ++st) { wqf[st] = trfrag(L + L_WQ + vtr0 + st * 8192, 4096); wqf[4 + st] = trfrag(L + L_WQ + vtr1 + st * 8192, 4096); }
  MEMBAR();
  *progw = 4 * 8u;
  lds_wait_ge<0>(vbar + 1, 4);
  {
    f32x4 a0 = {0.f, 0.f, 0.f, 0.f}, a1 = a0;
#pragma unroll
    for (int st = 0; st < 4; ++st) {
      const v4u hf = *(LAS v4u*)(L + L_HV + (n & 1) * 256 + st * 64 + g * 16);
      a0 = mfma16(wqf[st], hf, a0); a1 = mfma16(wqf[4 + st], hf, a1);
    }
    if (n < 4)
      __builtin_amdgcn_raw_buffer_store_b128(nosent4(n < 2 ? a0 : a1), wsr,
                                             WS_QP + (((n & 1) * 4 + (r >> 3)) * 8 + x) * 4096 + ((r & 7) * 128 + 32 * w + 16 * (n >> 1) + 4 * g) * 4, 0, AUX_SC1);
  }
  ST(5);
  {
    wait_full(G_MR + 8);
    f32x4 a0 = {0.f, 0.f, 0.f, 0.f}, a1 = a0;
#pragma unroll
    for (int st = 0; st < 4; ++st) {
      a0 = mfma16(trfrag(L + L_MRS + vtr0 + st * 8192, 4096), sf[st], a0);
      a1 = mfma16(trfrag(L + L_MRS + vtr1 + st * 8192, 4096), sf[st], a1);
    }
    LGKM0();
    *progw = 4 * (16u + 2 * w);
    if (n < 4)
      __builtin_amdgcn_raw_buffer_store_b128(nosent4(n < 2 ? a0 : a1), wsr,
                                             WS_MP + ((x + 8 * combo) * 8 + ks) * 1024 + (n & 1) * 512 + (32 * w + 16 * (n >> 1) + 4 * g) * 4, 0, AUX_SC1);
  }

  int kofs[4];
#pragma unroll
  for (int st = 0; st < 4; ++st) kofs[st] = n * 256 + (((4 * st + g) ^ n) << 4);
  v4u kf[2][8];
  auto kread = [&](int t, v4u (&k)[8]) {
    wait_full(G_K + 2 * t + 2);
    LAS unsigned char* kb = L + ((2 * t) & 31) * 4096;
#pragma unroll
    for (int st = 0; st < 4; ++st) { k[st] = *(LAS v4u*)(kb + kofs[st]); k[4 + st] = *(LAS v4u*)(kb + 4096 + kofs[st]); }
    MEMBAR();
    *progw = 4 * (16u + min(2 * t + 8, 64));
  };
  kread(w, kf[0]);
  ST(6);
  const int oa = WS_OR + (kvh_t * 32 + (lane >> 3)) * 2048 + u_t * 512 + w * 128 + (lane & 7) * 16;
  v4u s0, s1;
#pragma unroll
  for (int d = 0; d < 4; ++d) {
    const unsigned one = d == ((n & 7) >> 1) ? 0x3F80u << (16 * (n & 1)) : 0u;
    s0[d] = n < 8 ? one : 0u; s1[d] = n < 8 ? 0u : one;
  }
  const unsigned gw = G_WO + 2 * w + 2;
  LAS unsigned char* wa = L + w * 8192 + (8 * g + tq) * 128 + (tp & 1) * 8;
  const int ph = 2 * ((tq >> 1) | ((g & 1) << 1));
  int ost = WS_OR + (x * 32 + r) * 2048 + (2 * w + (n >> 3)) * 256 + g * 64 + (n & 7) * 8, oa_ = oa, wa4[4];
#pragma unroll
  for (int cb = 0; cb < 4; ++cb) wa4[cb] = (int)(size_t)(wa + (((2 * cb + (tp >> 1)) ^ ph) << 4));
  int ovw = l0 + L_OV + w * 256 + (g >> 1) * 128 + (n & 7) * 16 + (g & 1) * 8, ovr = l0 + L_OV + w * 256 + (n & 1) * 128 + g * 16;
  int t1w = l0 + L_T1R + (w * 128 + lane) * 4, t1r = l0 + L_T1R + lane * 8;
  int apst = WS_AP + ((lane >> 5) * 16 + kvh_t * 4 + u_t) * 2048 + (64 * cg_t + 2 * (lane & 31)) * 2;
  asm volatile("" : "+v"(s0), "+v"(s1), "+v"(ost), "+v"(oa_), "+v"(wa4[0]), "+v"(wa4[1]), "+v"(wa4[2]), "+v"(wa4[3]),
               "+v"(ovw), "+v"(ovr), "+v"(t1w), "+v"(t1r), "+v"(apst));
  {
    const int o2 = WS_QP + (x * 8 + (lane >> 5)) * 4096 + (w * 256 + 4 * (lane & 31)) * 4;
    v4u pq[8];
    __builtin_amdgcn_s_sleep(DLY2);
    lds_wait_ge<0>(ctrl + C_RDY, 2);
    if (TQ) {
      const unsigned tq_ = __builtin_amdgcn_readfirstlane(ctrl[C_TE]) + TQ;
      _Pragma("nounroll") for (int i = 0; i < 256 && (int)((unsigned)rt() - tq_) < 0; ++i) __builtin_amdgcn_s_sleep(1);
    }
    ST(10);
    unsigned need = 255u;
    _Pragma("nounroll") for (unsigned i = 0; i < GCAP; ++i) {
      MEMBAR();
#pragma unroll
      for (int k = 0; k < 8; ++k)
        if (need & (1u << k)) pq[k] = __builtin_amdgcn_raw_buffer_load_b128(wsr, o2 + (k & 1) * 512, (k >> 1) * 8192, AUX_SC1);
      MEMBAR();
      if (i == 0) kread(w + 4, kf[1]);
#pragma unroll
      for (int k = 0; k < 8; ++k)
        if ((need & (1u << k)) && __all(umax4(pq[k], 0) != SENT)) need &= ~(1u << k);
      STN(20, i + 1);
      if (!need) break;
      __builtin_amdgcn_s_sleep(4);
    }
    const f32x4 s0 = (asf(pq[0]) + asf(pq[2])) + (asf(pq[4]) + asf(pq[6])), s1 = (asf(pq[1]) + asf(pq[3])) + (asf(pq[5]) + asf(pq[7]));
    *(LAS v2u*)(L + L_QV + (2 * w + (lane >> 5)) * 272 + (lane & 31) * 8) =
        v2u{cvtpk(swap32_add(s0[0], s1[0]), swap32_add(s0[1], s1[1])), cvtpk(swap32_add(s0[2], s1[2]), swap32_add(s0[3], s1[3]))};
  }
  ST(7);
  lds_arrive(L, 2);
  lds_wait_ge<0>(vbar + 2, 4);
  v4u qf[4];
#pragma unroll
  for (int st = 0; st < 4; ++st) qf[st] = *(LAS v4u*)(L + L_QV + (n & 7) * 272 + st * 64 + g * 16);
  constexpr float SCALE = 0.08838834764831845f, LOG2E = 1.4426950408889634f;
  const int pofs = L_P + g * 128 + (n & 7) * 16;
  float mx = -INFINITY;
  auto qk = [&](int t, v4u (&k)[8]) {
    f32x4 s0 = {0.f, 0.f, 0.f, 0.f}, s1 = s0;
#pragma unroll
    for (int st = 0; st < 4; ++st) { s0 = mfma16(k[st], qf[st], s0); s1 = mfma16(k[4 + st], qf[st], s1); }
    if (t < 24) kread(t + 8, k);
    mx = fmaxf(fmaxf(fmaxf(mx, s0[0]), fmaxf(s0[1], s0[2])), fmaxf(fmaxf(s0[3], s1[0]), fmaxf(fmaxf(s1[1], s1[2]), s1[3])));
    *(LAS v4u*)(L + pofs + t * 512) = v4u{cvtpk(rbf(f32x2{s0[0], s0[1]}) * SCALE), cvtpk(rbf(f32x2{s0[2], s0[3]}) * SCALE),
                                          cvtpk(rbf(f32x2{s1[0], s1[1]}) * SCALE), cvtpk(rbf(f32x2{s1[2], s1[3]}) * SCALE)};
  };
#pragma unroll 1
  for (int t = w; t < 32; t += 8) { qk(t, kf[0]); qk(t + 4, kf[1]); }
  ST(8);
  mx = xrow_max(mx);
  if (lane < 8) *(LAS float*)(L + L_MX + lane * 16 + w * 4) = mx;
  lds_arrive(L, 3);
  lds_wait_ge<0>(vbar + 3, 4);
  {
    const f32x4 a = *(LAS f32x4*)(L + L_MX + (lane & 7) * 16);
    const float Mw = rbf(rbf(fmaxf(fmaxf(a[0], a[1]), fmaxf(a[2], a[3]))) * SCALE) * LOG2E;
    f32x2 ls = {0.f, 0.f};
    LAS v4u* pp = (LAS v4u*)(L + L_P + w * 4096 + lane * 16);
    v4u sc[4];
#pragma unroll
    for (int i = 0; i < 4; ++i) sc[i] = pp[64 * i];
#pragma unroll
    for (int i = 0; i < 4; ++i) {
#pragma unroll
      for (int k = 0; k < 4; ++k) {
        const f32x2 z = __builtin_elementwise_fma(unpk(sc[i][k]), f32x2{LOG2E, LOG2E}, f32x2{-Mw, -Mw});
        const f32x2 e = {__builtin_amdgcn_exp2f(z[0]), __builtin_amdgcn_exp2f(z[1])};
        ls += e;
        sc[i][k] = cvtpk(e);
      }
      pp[64 * i] = sc[i];
    }
    float l1 = ls[0] + ls[1];
    l1 += dpp<ROR8>(l1);
    l1 = xrow_sum(l1);
    if (lane < 8) *(LAS float*)(L + L_LS + lane * 16 + w * 4) = l1;
    lds_arrive(L, 4);
    lds_wait_ge<0>(vbar + 4, 4);
    if (w == 0) {
      const f32x4 b = *(LAS f32x4*)(L + L_LS + (lane & 7) * 16);
      if (lane < 8) {
        __builtin_amdgcn_raw_buffer_store_b64(v2u{nosent(__float_as_uint(Mw)), nosent(__float_as_uint((b[0] + b[1]) + (b[2] + b[3])))}, wsr, WS_ST + (x * 32 + r) * 64 + lane * 8, 0, AUX_SC1);
        *(LAS float*)(L + L_FH + lane * 4) = Mw;
      }
      LGKM0();
      ctrl[lane == 0 ? C_STAT : C_DUMMY + lane] = 1u;
    }
  }
  const int col = 256 * w + 8 * n + 2 * g;
  unsigned fx[2], fm[8];
  ST(9);

  v4u sel;
  f32x4 cb1, ux, uga, usb, ugb;
  int lnw, lnr, outo;
  int ob = WS_AP + (j * 16 + 4 * g) * 2048 + (256 * w + 8 * n) * 2;
  f32x4 o0 = {0.f, 0.f, 0.f, 0.f}, o1 = o0;
  float fh = 0.f;
#pragma unroll 1
  for (int t = 0; t < 32; ++t) {
    const v4u b = *(LAS v4u*)(L + pofs + t * 512);
    wait_full(G_V + 2 * t + 2);
    LAS unsigned char* vb = L + ((2 * t) & 31) * 4096;
    const v4u a0 = trfrag(vb + vtr0, 4096), a1 = trfrag(vb + vtr1, 4096);
    MEMBAR();
    *progw = 4 * (16u + 64 + 2 * t + 2);
    o0 = mfma16(a0, b, o0); o1 = mfma16(a1, b, o1);
    if (t == 24) {
      if (j < 2 && w == 0) {
        u64 pc1; int o1, o2;
        asm volatile("s_getpc_b64 %0\n.Lfb_here:\n\ts_mov_b32 %1, .Lfb_fin-.Lfb_here\n\ts_mov_b32 %2, .Lfb_end2-.Lfb_here" : "=s"(pc1), "=s"(o1), "=s"(o2));
        if (o2 > o1 && o1 > 0)
          asm volatile("s_mov_b32 m0, %2\n\ts_nop 0\n\tglobal_load_lds_dword %1, %0" :: "s"(pc1), "v"(min(o1 + lane * 64, o2 - 4) & ~3), "s"(l0 + L_BYTES - 256) : "memory");
      }
  if (j < 2) {
#pragma unroll
    for (int k = 0; k < 2; ++k) fx[k] = *(const unsigned*)(xin + j * D + col + 128 * k);
#pragma unroll
    for (int k = 0; k < 8; ++k) fm[k] = __builtin_amdgcn_raw_buffer_load_b32(wsr, WS_MR + j * 8192 + col * 2 + (k >> 1) * 2048 + (k & 1) * 256, 0, AUX_SC1);
  }
    }
    if (t == 30) {
      lds_wait_ge<0>(ctrl + C_FH, 1);
      fh = *(LAS float*)(L + L_FH + (n & 7) * 4);
    }
  }
  ST(13);
  {
    const f32x4 o = (n < 8 ? o0 : o1) * fh;
    __builtin_amdgcn_raw_buffer_store_b64(v2u{nosent(cvtpk(o[0], o[1])), nosent(cvtpk(o[2], o[3]))}, wsr, ost, 0, AUX_SC1);
  }
  {
    if (j < 2) {
    _Pragma("nounroll") for (unsigned i = 0; i < GCAP; ++i) {
      if (__all(umax4(v4u{fm[0], fm[1], fm[2], fm[3]}, umax4(v4u{fm[4], fm[5], fm[6], fm[7]}, 0)) != SENT)) break;
      MEMBAR();
#pragma unroll
      for (int k = 0; k < 8; ++k) fm[k] = __builtin_amdgcn_raw_buffer_load_b32(wsr, WS_MR + j * 8192 + col * 2 + (k >> 1) * 2048 + (k & 1) * 256, 0, AUX_SC1);
    }
#pragma unroll
    for (int d = 0; d < 4; ++d) sel[d] = (d == (n >> 2) && !(n & 2)) ? (0x3F80u << (16 * (n & 1))) : 0u;
      cb1 = rbf(1.f + unpk4(v2u{fm[4], fm[5]}));
      ux = unpk4(v2u{fx[0], fx[1]}); uga = unpk4(v2u{fm[0], fm[1]}); usb = unpk4(v2u{fm[2], fm[3]}); ugb = unpk4(v2u{fm[6], fm[7]});
      lnw = l0 + (lane == 63 ? L_LNR + 4 * w : L_T1R + 3072 - 512 + 4 * lane); lnr = l0 + L_LNR; outo = (j * D + col) * 2;
      asm volatile("" : "+v"(ux), "+v"(uga), "+v"(usb), "+v"(ugb), "+v"(cb1), "+v"(lnw), "+v"(lnr), "+v"(outo));
      asm volatile("" : "+v"(sel), "+v"(ob));
      __builtin_amdgcn_s_sleep(DLYA > 6 ? DLYA - 6 : 0);
    } else {
      __builtin_amdgcn_s_sleep(DLYA);
    }
    ST(14);
    v4u wf[8], of[8];
    auto wread = [&]() {
#pragma unroll
      for (int cb = 0; cb < 4; ++cb) { LAS unsigned char* ac = (LAS unsigned char*)(size_t)wa4[cb]; wf[cb] = trfrag(ac, 512); wf[4 + cb] = trfrag(ac + 4096, 512); }
    };
    MEMBAR();
#pragma unroll
    for (int k = 0; k < 8; ++k) of[k] = __builtin_amdgcn_raw_buffer_load_b128(wsr, oa_, (k >> 2) * (128 * 2048) + (k & 3) * (8 * 2048), AUX_SC1);
    MEMBAR();
    fullc = __builtin_amdgcn_readfirstlane(ctrl[C_FULL]);
    const bool havew = fullc >= 4 * gw;
    if (havew) { MEMBAR(); wread(); }
    f32x4 a0 = {0.f, 0.f, 0.f, 0.f}, a1 = a0;
    unsigned mx8 = 0;
#pragma unroll
    for (int k = 0; k < 8; ++k) {
      if (k < 4) a0 = mfma16(s0, of[k], a0); else a1 = mfma16(s1, of[k], a1);
      mx8 = umax4(of[k], mx8);
    }
    if (!__all(mx8 != SENT)) {
      _Pragma("nounroll") for (unsigned i = 0; i < GCAP; ++i) {
        unsigned need = 0;
#pragma unroll
        for (int k = 0; k < 8; ++k) need |= __all(umax4(of[k], 0) != SENT) ? 0u : 1u << k;
        STN(21, i + 1);
        if (!need) break;
        MEMBAR();
#pragma unroll
        for (int k = 0; k < 8; ++k)
          if (need & (1u << k)) of[k] = __builtin_amdgcn_raw_buffer_load_b128(wsr, oa_, (k >> 2) * (128 * 2048) + (k & 3) * (8 * 2048), AUX_SC1);
      }
      a0 = a1 = f32x4{0.f, 0.f, 0.f, 0.f};
#pragma unroll
      for (int k = 0; k < 4; ++k) { a0 = mfma16(s0, of[k], a0); a1 = mfma16(s1, of[4 + k], a1); }
    }
    const f32x4 os = a0 + a1;
    ST(12);
    *(LAS v2u*)(size_t)ovw =
        v2u{cvtpk(os[0] + dpp<ROR8>(os[0]), os[1] + dpp<ROR8>(os[1])), cvtpk(os[2] + dpp<ROR8>(os[2]), os[3] + dpp<ROR8>(os[3]))};
    if (!havew) { wait_full(gw); wread(); }
    MEMBAR();
    const v4u oa0 = *(LAS v4u*)(size_t)ovr, oa1 = *(LAS v4u*)(size_t)(ovr + 64);
    f32x4 acc[4];
#pragma unroll
    for (int cb = 0; cb < 4; ++cb) acc[cb] = mfma16(oa1, wf[4 + cb], mfma16(oa0, wf[cb], f32x4{0.f, 0.f, 0.f, 0.f}));
    ST(15);
    LAS float* t1 = (LAS float*)(size_t)t1w;
    t1[0] = (g & 2) ? ((g & 1) ? acc[3][0] : acc[2][0]) : ((g & 1) ? acc[1][0] : acc[0][0]);
    t1[64] = (g & 2) ? ((g & 1) ? acc[3][1] : acc[2][1]) : ((g & 1) ? acc[1][1] : acc[0][1]);
  }
  LGKM0();
  __builtin_amdgcn_s_barrier();
  MEMBAR();
  if (w == 0) {
    LAS f32x2* t1 = (LAS f32x2*)(size_t)t1r;
    __builtin_amdgcn_raw_buffer_store_b32(nosent(cvtpk((t1[0] + t1[64]) + (t1[128] + t1[192]))), wsr, apst, 0, AUX_SC1);
  }
  ST(16);
  if (j >= 2) { STDUMP(); return; }

  asm volatile(".Lfb_fin:");
  lds_wait_ge<0>(ctrl + C_TB, 1);
  __builtin_amdgcn_s_sleep(DLYB);
  f32x4 at[2];
  {
    _Pragma("nounroll") for (unsigned i = 0; i < GCAP; ++i) {
      MEMBAR();
      v4u ap[8];
#pragma unroll
      for (int k = 0; k < 8; ++k) ap[k] = __builtin_amdgcn_raw_buffer_load_b128(wsr, ob + (k >> 2) * 256, (k & 3) * 2048, AUX_SC1);
      unsigned m = 0;
#pragma unroll
      for (int k = 0; k < 8; ++k) { m = umax4(ap[k], m); at[k >> 2] = mfma16(sel, ap[k], (k & 3) ? at[k >> 2] : f32x4{0.f, 0.f, 0.f, 0.f}); }
      if (__all(m != SENT)) break;
    }
  }
  ST(17);
  auto layernorm4 = [&](f32x4 v, int slot) -> v2u {
    const float piv = __builtin_bit_cast(float, __builtin_amdgcn_readfirstlane(__builtin_bit_cast(int, v[0])));
    const f32x4 d = v - piv;
    const f32x2 d0 = {d[0], d[1]}, d1 = {d[2], d[3]}, sd = d0 + d1, sq = __builtin_elementwise_fma(d0, d0, d1 * d1);
    float s1 = sd[0] + sd[1], s2 = sq[0] + sq[1];
    sum2_to_last(s1, s2);
    LAS float* e = (LAS float*)(size_t)(lnw + slot * 32);
    e[0] = fmaf(s1, 1.f / 256.f, piv); e[4] = fmaf(s1 * (-1.f / 256.f), s1, s2);
    LGKM0();
    __builtin_amdgcn_s_barrier();
    MEMBAR();
    const f32x4 em = *(LAS f32x4*)(size_t)(lnr + slot * 32), eq = *(LAS f32x4*)(size_t)(lnr + slot * 32 + 16);
    const f32x2 ma = {em[0], em[1]}, mb = {em[2], em[3]}, ms = ma + mb, qs = f32x2{eq[0], eq[1]} + f32x2{eq[2], eq[3]};
    const float m = (ms[0] + ms[1]) * 0.25f;
    const f32x2 da = ma - m, db = mb - m, dq = __builtin_elementwise_fma(__builtin_elementwise_fma(da, da, db * db), f32x2{256.f, 256.f}, qs);
    const f32x4 c = rbf(v - rbf(m));
    const float rs = __builtin_amdgcn_rcpf(rbf(__builtin_amdgcn_sqrtf(rbf(rbf((dq[0] + dq[1]) * (1.f / 1024.f)) + 1e-5f))));
    return pk4(c * rs);
  };
  const f32x4 x1 = rbf(ux + rbf(uga * rbf(f32x4{at[0][0], at[0][1], at[1][0], at[1][1]})));
  const v2u hp = layernorm4(rbf(rbf(unpk4(layernorm4(x1, 0)) * cb1) + usb), 1);
  float yv[4];
  typedef unsigned short u16x2 __attribute__((ext_vector_type(2)));
  u16x2 mg[2];
#pragma unroll
  for (int k = 0; k < 2; ++k) {
    mg[k] = __builtin_bit_cast(u16x2, hp[k] & 0x7fff7fffu);
    const u16x2 ii = __builtin_elementwise_min(__builtin_elementwise_sub_sat(mg[k], u16x2{TB0, TB0}), u16x2{TBN - 1, TBN - 1});
    const unsigned o = (__builtin_bit_cast(unsigned, ii) << 2) | ((hp[k] >> 14) & 0x00020002u);
    yv[2 * k] = bflo(*(LAS u16*)(L + L_TB + (o & 0xffffu)));
    yv[2 * k + 1] = bflo(*(LAS u16*)(L + L_TB + (o >> 16)));
  }
  const u16x2 lo = __builtin_elementwise_min(mg[0] - u16x2{1, 1}, mg[1] - u16x2{1, 1}), hi = __builtin_elementwise_max(mg[0], mg[1]);
  const bool miss = min(lo[0], lo[1]) < TB0 || max(hi[0], hi[1]) >= TB0 + TBN;
  if (__builtin_expect(__any(miss), 0)) {
#pragma unroll 1
    for (int k = 0; k < 4; ++k) {
      const float hk = k == 0 ? bflo(hp[0]) : k == 1 ? bfhi(hp[0]) : k == 2 ? bflo(hp[1]) : bfhi(hp[1]);
      const unsigned mag = (__float_as_uint(hk) >> 16) & 0x7fffu;
      u64 todo = __ballot(mag - (TB0 + 1) >= TBN - 1 && mag != 0u);
      while (todo) {
        const int src = __builtin_ctzll(todo);
        todo &= todo - 1;
        const float hs = __builtin_bit_cast(float, __builtin_amdgcn_readlane(__builtin_bit_cast(int, hk), src));
        float acc = 0.f;
        v4u a1 = ((const v4u*)w1)[lane], c1 = ((const v4u*)b1)[lane], a2 = ((const v4u*)w2)[lane];
#pragma unroll 1
        for (int i = 1; i <= 7; ++i) {
          const int nx = (i < 7 ? i : 0) * 64 + lane;
          const v4u n1 = ((const v4u*)w1)[nx], m1 = ((const v4u*)b1)[nx], n2 = ((const v4u*)w2)[nx];
#pragma unroll
          for (int q = 0; q < 4; ++q) acc = gelu_term(hs, bfhi(a1[q]), bfhi(c1[q]), bfhi(a2[q]), gelu_term(hs, bflo(a1[q]), bflo(c1[q]), bflo(a2[q]), acc));
          a1 = n1; c1 = m1; a2 = n2;
        }
        const float y = rbf(rbf(wave_sum(acc)) + bflo(b2[0]));
        if (lane == src) { yv[0] = k == 0 ? y : yv[0]; yv[1] = k == 1 ? y : yv[1]; yv[2] = k == 2 ? y : yv[2]; yv[3] = k == 3 ? y : yv[3]; }
      }
    }
  }
  const v2u res = pk4(x1 + rbf(ugb * f32x4{yv[0], yv[1], yv[2], yv[3]}));
  *(unsigned*)((char*)out + outo) = res[0];
  *(unsigned*)((char*)out + outo + 256) = res[1];
  ST(18);
  STDUMP();
  asm volatile(".Lfb_end2:");
  return;
  }
  {
    __builtin_amdgcn_s_setprio(3);
    const unsigned t_load = (unsigned)rt();
    const int q4 = g, c16 = n;
    unsigned X;
    {
      const v4u s4 = {SENT, SENT, SENT, SENT};
      const unsigned c0 = WS_CLR + j * CLR_PER_WG + lane * 16;
      const unsigned c1 = lane < 28 ? c0 + 2048
                        : WS_P1 + ((x * 8 + ks) * 2 + (combo >> 1)) * 1024 + (lane >> 4 & 1) * 512 + (combo & 1) * 256 + (lane & 15) * 16;
      const unsigned cq = WS_QP + (((lane >> 5) * 4 + (r >> 3)) * 8 + x) * 4096 + (r & 7) * 512 + (lane & 31) * 16;
      const unsigned cm = WS_MP + ((x + 8 * combo) * 8 + ks) * 1024 + lane * 16;
      asm volatile("global_load_dword %0, %7, %6 sc1\n\t"
                   "global_store_dwordx4 %2, %5, %6 sc1\n\tglobal_store_dwordx4 %3, %5, %6 sc1\n\tglobal_store_dwordx4 %4, %5, %6 sc1\n\t"
                   "global_store_dwordx4 %1, %5, %6 sc1\n\tglobal_store_dwordx4 %1, %5, %6 offset:1024 sc1\n\ts_nop 1"
                   : "=&v"(X) : "v"(c0), "v"(c1), "v"(cq), "v"(cm), "v"(s4), "s"(ws), "v"(WS_X) : "memory");
    }
    const unsigned fwaddr = l0 + L_CTRL + 4 * (lane == 0 ? C_FULL : C_DUMMY + lane), pgaddr = l0 + L_CTRL + 4 * (C_PROG + (lane & 3));
    int P = 0, freed = 0;
    unsigned due = (t_load - 1000) * 16, now = t_load * 16, nxt = now, cvm = 0;
    {
      const int r8 = lane >> 3, c8 = lane & 7;
      constexpr unsigned ps = 8 * 12288 - 1024;
      const unsigned v0 = r8 * 12288 + ((c8 ^ (2 * ((r8 >> 1) & 3))) << 4);
      stream_groups<4, false>((const char*)wmod + (size_t)(ks * 128) * 12288 + ((combo >> 1) * 1024 + x * 128 + (combo & 1) * 64) * 2, 32 * 12288, l0 + L_P, 4,
                                P, due, now, nxt, cvm, freed, v0, v0 + ps, v0 + 2 * ps, v0 + 3 * ps, fwaddr, pgaddr, 0, DELTA16, W0, 0);
    }
    const unsigned sw0 = (c16 ^ (2 * q4)) << 4, sw1 = (c16 ^ (2 * (4 + q4))) << 4;
    {
      constexpr unsigned ps = 4 * 8192 - 1024;
      const unsigned v0 = q4 * 8192 + sw0, v1 = q4 * 8192 + sw1 + ps;
      const char* sb = (const char*)wq + (size_t)(x * 128) * 8192 + r * 256;
      if (B2) stream_groups<4, false>(sb, 16 * 8192, l0 + L_WQ, B2, P, due, now, nxt, cvm, freed, v0, v1, v0 + 2 * ps, v1 + 2 * ps, fwaddr, pgaddr, 0, DELTA16, W0, 0);
      asm volatile("s_waitcnt vmcnt(%1)" : "+v"(X) : "i"(16 + 4 * B2) : "memory");
      const unsigned tag = X * 1664525u + 1013904223u;
      ctrl[lane == 0 ? C_TAG : C_DUMMY + lane] = tag;
      ctrl[lane == 0 ? C_CLR : C_DUMMY + lane] = 1u;
      asm volatile("s_mov_b64 exec, 3\n\tglobal_store_dwordx2 %0, %1, %2 sc1\n\ts_mov_b64 exec, -1"
                   :: "v"(lane == 0 ? WS_RDG + (x * 32 + r) * 16 : WS_RDY + (x * 32 + r) * 32), "v"(v2u{tag, t_load}), "s"(ws) : "memory");
      ST(6);
      due = (t_load + ANCHOR) * 16 + (4 + B2) * DELTA0 + ((PH & 64 ? (r * 13 + x * 5) & 31 : r) * (PH & 63) * DELTA16) / 256;
      stream_groups<4, false>(sb + B2 * 16 * 8192, 16 * 8192, l0 + L_WQ + B2 * 4096, 8 - B2, P, due, now, nxt, cvm, freed, v0, v1, v0 + 2 * ps, v1 + 2 * ps, fwaddr, pgaddr);
    }
    {
      constexpr unsigned ps = 4 * 12288 - 1024;
      const unsigned v0 = q4 * 12288 + sw0, v1 = q4 * 12288 + sw1 + ps;
      stream_groups<4, false>((const char*)wmod + (size_t)(ks * 128) * 12288 + 4096 + (x + 8 * combo) * 256, 16 * 12288, l0 + L_MRS, 8,
                                P, due, now, nxt, cvm, freed, v0, v1, v0 + 2 * ps, v1 + 2 * ps, fwaddr, pgaddr);
    }
    const int r8w = lane >> 3, c8w = lane & 7;
    constexpr unsigned psw = 256 * 2048 - 1024;
    const unsigned row = (lane >> 5) * (128 * 2048) + (r8w & 3) * 2048;
    const unsigned w0 = row + ((c8w ^ (2 * ((r8w >> 1) & 1))) << 4), w1_ = row + ((c8w ^ (2 * (((r8w >> 1) & 1) | 2))) << 4) + psw;
    const char* wob = (const char*)wo + (size_t)(kvh_t * 1024 + 32 * u_t) * 2048 + cg_t * 128;
    const size_t kvbase = (size_t)x * SK * 256 + (size_t)r * 8192;
    unsigned dmin = DMIN;
    {
      const unsigned v0 = q4 * 256 + ((c16 ^ q4) << 4);
      const char* sb = (const char*)kc + kvbase + 4096;
      ST(12);
      stream_groups<8, true>(sb, 262144, l0 + 4096, KA, P, due, now, nxt, cvm, freed, v0, v0 ^ 64, v0 ^ 128, v0 ^ 192, fwaddr, pgaddr, 4 * (G_K + 14));
      ST(13);
      const unsigned t0 = __builtin_amdgcn_readfirstlane(ctrl[C_T0]);
      if (t0 && (unsigned)(t0 - t_load + 400) < 800u) {
        due += (t0 - t_load) * 16;
        if ((int)(t_load - t0) > 50) dmin = DMINL;
      }
      const unsigned pk1 = stream_groups<8, true>(sb + KA * 262144, 262144, l0 + KA * 8192 + 4096, KSLOW - KA, P, due, now, nxt, cvm, freed, v0, v0 ^ 64, v0 ^ 128, v0 ^ 192, fwaddr, pgaddr,
                             4 * (G_K + 14), DELTA16, W0, dmin, 4 * G_K + 8 * KW);
      ST(14);
      const unsigned pk2 = stream_groups<8, true>(sb + KSLOW * 262144, 262144, l0 + ((KSLOW * 8192 + 4096) & 131071), 32 - KSLOW, P, due, now, nxt, cvm, freed, v0, v0 ^ 64, v0 ^ 128, v0 ^ 192,
                             fwaddr, pgaddr, 4 * (G_K + 14), DELTAV, W0E, dmin);
      STN(21, pk1); STN(22, pk2);
    }
    ST(16);
    {
      const unsigned v0 = q4 * 256 + sw0;
      stream_groups<8, true>((const char*)vc + kvbase + 4096, 262144, l0 + 4096, 32, P, due, now, nxt, cvm, freed, v0, v0 ^ 128, v0, v0 ^ 128, fwaddr, pgaddr, 4 * (G_K + 14), DELTAV, W0E, dmin);
    }
    ST(3); ST(18);
    due += WOGAP * 16;
    stream_groups<4, true>(wob, 4 * 2048, l0, 8, P, due, now, nxt, cvm, freed, w0, w1_, w0 + 2 * psw, w1_ + 2 * psw, fwaddr, pgaddr, 4 * (G_K + 15), DELTAW, W0E);
    stream_drain(P, fwaddr);
    asm volatile("s_waitcnt vmcnt(0)" ::: "memory");
    ctrl[lane == 0 ? C_FULL : C_DUMMY + lane] = 4 * G_END;
    ST(4);
    STDUMP();
  }

}

}

extern "C" void kernel_launch(void* const* d_in, const int* in_sizes, int n_in, void* d_out, int out_size, void* d_ws, size_t ws_size,
                              hipStream_t stream) {
  (void)hipFuncSetAttribute((const void*)fused_block, hipFuncAttributeMaxDynamicSharedMemorySize, L_BYTES + STAMP * 1152);
  fused_block<<<256, 576, L_BYTES + STAMP * 1152, stream>>>((const u16*)d_in[0], (const u16*)d_in[1], (const u16*)d_in[2], (const u16*)d_in[3],
                                             (const u16*)d_in[4], (const u16*)d_in[6], (const u16*)d_in[7], (const u16*)d_in[8],
                                             (const u16*)d_in[9], (const u16*)d_in[10], (const u16*)d_in[11], (u16*)d_out,
                                             (unsigned char*)d_ws);
}
```
